```python
import torch

def _softmax(x, axis=-1):
    return torch.softmax(x, dim=axis)
def _gelu(x):
    return torch.nn.functional.gelu(x, approximate="tanh")
def _silu(x):
    return torch.nn.functional.silu(x)
def _sigmoid(x):
    return torch.sigmoid(x)
def _layernorm(x, axis=-1, eps=1e-5):
    m = x.mean(dim=axis, keepdim=True)
    v = x.var(dim=axis, keepdim=True, unbiased=False)
    return (x - m) / torch.sqrt(v + eps)
def _scatter_add(tgt, idx, src, axis=0):
    return tgt.clone().index_add_(axis, idx, src)
def _segment_sum(src, seg, n):
    out = torch.zeros((n,) + src.shape[1:], dtype=src.dtype)
    return out.index_add_(0, seg, src)
def _pad(x, pw):
    flat = tuple(v for pair in reversed(pw) for v in pair)
    return torch.nn.functional.pad(x, flat)

kernel_name = 'standalone__gqa_block_cached__near_ridge'

def setup_inputs():

    _w = (lambda a: (lambda t: t.to(torch.bfloat16) if t.is_floating_point() else t)(torch.as_tensor(a)))
    B, Sq, D = 8, 112, 640
    Hq, Hkv, Sk, Dh = 32, 32, 512, 128
    assert Hq % Hkv == 0, (Hq, Hkv)
    return {
        "x": _w((lambda *s: torch.randn(*s, dtype=torch.bfloat16) * 0.8)(B, Sq, D)),
        "k_cache": _w((lambda *s: torch.randn(*s, dtype=torch.bfloat16) * 0.8)(B, Hkv, Sk, Dh)),
        "v_cache": _w((lambda *s: torch.randn(*s, dtype=torch.bfloat16) * 0.05)(B, Hkv, Sk, Dh)),
        "wq": _w((lambda *s: torch.randn(*s, dtype=torch.bfloat16) * 0.8)(D, Hq * Dh) * (D ** -0.5)),
        "wo": _w((lambda *s: torch.randn(*s, dtype=torch.bfloat16) * 0.05)(Hq * Dh, D) * ((Hq * Dh) ** -0.5)),
    }

def reference(x, k_cache, v_cache, wq, wo):
    B, Sq, D = x.shape
    Hq, Dh = 32, 128
    Hkv = k_cache.shape[1]
    rep = Hq // Hkv
    q = (lambda x, *d: x.permute(*d))((x @ wq).reshape(B, Sq, Hq, Dh), 0, 2, 1, 3)
    k = (lambda x, n, axis: torch.repeat_interleave(x, n, dim=axis))(k_cache, rep, axis=1)
    v = (lambda x, n, axis: torch.repeat_interleave(x, n, dim=axis))(v_cache, rep, axis=1)
    scores = torch.einsum("bhqd,bhkd->bhqk", q, k) * (Dh ** -0.5)
    p = _softmax(scores, axis=-1)
    o = torch.einsum("bhqk,bhkd->bhqd", p, v)
    return (lambda x, *d: x.permute(*d))(o, 0, 2, 1, 3).reshape(B, Sq, Hq * Dh) @ wo
```

```cpp
#include <hip/hip_runtime.h>
#include <hip/hip_bf16.h>
typedef __hip_bfloat16 bf16;
constexpr int B=8,SQ=112,D=640,H=32,SK=512,DH=128;
__global__ void qproj(const bf16* x,const bf16* wq,bf16* q){
  int n = blockIdx.x*256+threadIdx.x; int m = blockIdx.y;
  float acc=0; for(int k=0;k<D;k++) acc += __bfloat162float(x[m*D+k])*__bfloat162float(wq[(size_t)k*4096+n]);
  q[(size_t)m*4096+n]=__float2bfloat16(acc);
}
__global__ void attn(const bf16* q,const bf16* kc,const bf16* vc,bf16* o){
  int row=blockIdx.x, h=blockIdx.y, b=blockIdx.z; int t=threadIdx.x;
  __shared__ float qs[DH]; __shared__ float s[SK]; __shared__ float red[128];
  qs[t]=__bfloat162float(q[(size_t)(b*SQ+row)*4096+h*DH+t]); __syncthreads();
  const bf16* K=kc+(size_t)(b*H+h)*SK*DH; const bf16* V=vc+(size_t)(b*H+h)*SK*DH;
  float mx=-1e30f;
  for(int j=t;j<SK;j+=128){ float a=0; for(int d=0;d<DH;d++) a+=qs[d]*__bfloat162float(K[j*DH+d]); a*=0.08838834764831845f; s[j]=a; mx=fmaxf(mx,a);}
  red[t]=mx; __syncthreads(); for(int o2=64;o2>0;o2>>=1){ if(t<o2) red[t]=fmaxf(red[t],red[t+o2]); __syncthreads(); } mx=red[0]; __syncthreads();
  float sum=0; for(int j=t;j<SK;j+=128){ float p=__expf(s[j]-mx); s[j]=p; sum+=p; }
  red[t]=sum; __syncthreads(); for(int o2=64;o2>0;o2>>=1){ if(t<o2) red[t]+=red[t+o2]; __syncthreads(); } sum=red[0];
  float a=0; for(int j=0;j<SK;j++) a+=s[j]*__bfloat162float(V[j*DH+t]);
  o[(size_t)(b*SQ+row)*4096+h*DH+t]=__float2bfloat16(a/sum);
}
__global__ void oproj(const bf16* o,const bf16* wo,bf16* out){
  int n = blockIdx.x*128+threadIdx.x; int m = blockIdx.y;
  float acc=0; for(int k=0;k<4096;k++) acc += __bfloat162float(o[(size_t)m*4096+k])*__bfloat162float(wo[(size_t)k*D+n]);
  out[(size_t)m*D+n]=__float2bfloat16(acc);
}
extern "C" void kernel_launch(void* const* d_in, const int* in_sizes, int n_in, void* d_out, int out_size, void* d_ws, size_t ws_size, hipStream_t stream) {
  const bf16* x=(const bf16*)d_in[0]; const bf16* kc=(const bf16*)d_in[1]; const bf16* vc=(const bf16*)d_in[2]; const bf16* wq=(const bf16*)d_in[3]; const bf16* wo=(const bf16*)d_in[4];
  bf16* q=(bf16*)d_ws; bf16* o=q+896*4096;
  qproj<<<dim3(16,896),256,0,stream>>>(x,wq,q);
  attn<<<dim3(SQ,H,B),128,0,stream>>>(q,kc,vc,o);
  oproj<<<dim3(5,896),128,0,stream>>>(o,wo,(bf16*)d_out);
}
```

```cpp
#include <hip/hip_runtime.h>
#include <hip/hip_bf16.h>
#include <stdint.h>

typedef __attribute__((ext_vector_type(8))) short bf16x8;
typedef __attribute__((ext_vector_type(4))) short s16x4;
typedef __attribute__((ext_vector_type(4))) float f32x4;
typedef __attribute__((ext_vector_type(4))) unsigned u32x4;
typedef __attribute__((ext_vector_type(2))) unsigned u32x2;
#define LDSP(T) __attribute__((address_space(3))) T*
#define DI __device__ __forceinline__

constexpr int NB = 8, SQ = 112, DM = 640, NH = 32, SK = 512, DH = 128;
constexpr int SLOT = 16384;

typedef __attribute__((ext_vector_type(2))) float f32x2;
typedef __attribute__((ext_vector_type(2))) __bf16 bf16x2;
DI unsigned cvtpk(float lo, float hi) {
  f32x2 v = {lo, hi};
  return __builtin_bit_cast(unsigned, __builtin_convertvector(v, bf16x2));
}
DI f32x4 mfma16(bf16x8 a, bf16x8 b, f32x4 c) { return __builtin_amdgcn_mfma_f32_16x16x32_bf16(a, b, c, 0, 0, 0); }
DI s16x4 trread(unsigned addr) { return __builtin_amdgcn_ds_read_tr16_b64_v4i16((LDSP(s16x4))(size_t)addr); }
DI bf16x8 trpair(unsigned a0, unsigned a1) {
  s16x4 lo = trread(a0), hi = trread(a1);
  return __builtin_shufflevector(lo, hi, 0, 1, 2, 3, 4, 5, 6, 7);
}
DI bf16x8 ldsread16(unsigned addr) { return *(LDSP(bf16x8))(size_t)addr; }
DI void ldswrite16(unsigned addr, bf16x8 v) { *(LDSP(bf16x8))(size_t)addr = v; }

template <int OFF> DI void gload(bf16x8& dst, unsigned voff, const void* sbase) {
  asm volatile("global_load_dwordx4 %0, %1, %2 offset:%3" : "=v"(dst) : "v"(voff), "s"(sbase), "i"(OFF) : "memory");
}
DI void dma2(unsigned m0val, unsigned voff, const void* sbase) {
  unsigned keep;
  asm volatile("s_mov_b32 %0, m0\n\ts_mov_b32 m0, %1\n\ts_nop 0\n\t"
               "global_load_lds_dwordx4 %2, %3\n\tglobal_load_lds_dwordx4 %2, %3 offset:1024\n\t"
               "s_mov_b32 m0, %0"
               : "=&s"(keep) : "s"(m0val), "v"(voff), "s"(sbase) : "memory");
}
DI void dma1(unsigned m0val, unsigned voff, const void* sbase) {
  unsigned keep;
  asm volatile("s_mov_b32 %0, m0\n\ts_mov_b32 m0, %1\n\ts_nop 0\n\t"
               "global_load_lds_dwordx4 %2, %3\n\t"
               "s_mov_b32 m0, %0"
               : "=&s"(keep) : "s"(m0val), "v"(voff), "s"(sbase) : "memory");
}
#define BARRIER() asm volatile("s_waitcnt lgkmcnt(0)\n\ts_barrier" ::: "memory")
template <int N> DI void waitv() { asm volatile("s_waitcnt vmcnt(%0)" ::"i"(N) : "memory"); }
template <int N> DI void waitv2(bf16x8& a, bf16x8& b) { asm volatile("s_waitcnt vmcnt(%2)" : "+v"(a), "+v"(b) : "i"(N) : "memory"); }
template <int N> DI void waitv4(bf16x8& a, bf16x8& b, bf16x8& c, bf16x8& d) {
  asm volatile("s_waitcnt vmcnt(%4)" : "+v"(a), "+v"(b), "+v"(c), "+v"(d) : "i"(N) : "memory");
}

__host__ __device__ constexpr int vslot(int t) { return t < 2 ? 8 + t : t - 2; }

template <bool CW>
DI void attn_body(const uint16_t* __restrict__ x, const uint16_t* __restrict__ kc, const uint16_t* __restrict__ vc,
                  const uint16_t* __restrict__ wq, uint16_t* __restrict__ ows, unsigned lds0, int wid, int lane, int b, int h) {
  const int r = lane & 15, g = lane >> 4;
  const int q4 = r >> 2, p = lane & 3;
  bf16x8 wr[10][2];
  bf16x8 xf[20];
  const unsigned wq_voff = (unsigned)(((8 * wid + g) * 4096 + 8 * r) * 2);
  const uint16_t* wq_s = wq + h * DH;
  const unsigned x_voff = (unsigned)((r * DM + 8 * g) * 2);
  const uint16_t* x_s = x + (size_t)(b * SQ + 16 * wid) * DM;
#pragma unroll
  for (int c = 0; c < 10; ++c) {
    gload<0>(wr[c][0], wq_voff, wq_s + (size_t)(64 * c) * 4096);
    gload<0>(wr[c][1], wq_voff, wq_s + (size_t)(64 * c + 4) * 4096);
    if constexpr (CW) {
      gload<0>(xf[2 * c], x_voff + 128 * c, x_s);
      gload<64>(xf[2 * c + 1], x_voff + 128 * c, x_s);
    }
  }
  const uint16_t* kb = kc + (size_t)(b * NH + h) * SK * DH;
  const uint16_t* vb = vc + (size_t)(b * NH + h) * SK * DH;
  const unsigned k_voff = (unsigned)(((8 * wid + g) * DH + 8 * (r ^ (g | ((wid & 3) << 2)))) * 2);
  const unsigned v_voff = (unsigned)(((8 * wid + g) * DH + 8 * (r ^ ((g | ((wid & 1) << 2)) << 1))) * 2);
#pragma unroll
  for (int t = 0; t < 8; ++t) dma2(lds0 + t * SLOT + wid * 2048, k_voff, kb + t * 64 * DH);

  const unsigned wq_waddr = lds0 + 8 * SLOT + (unsigned)((8 * wid + g) * 256 + ((r ^ ((g | ((wid & 1) << 2)) << 1)) << 4));
  const unsigned tr0 = lds0 + (unsigned)((8 * g + q4) * 256 + ((q4 | ((g & 1) << 2)) << 5) + ((p >> 1) << 4) + 8 * (p & 1));
  const unsigned kr0 = lds0 + (unsigned)((8 * (r >> 2) + (r & 3)) * 256 + ((g ^ r) << 4));

  f32x4 qa[8];
#pragma unroll
  for (int c = 0; c < 8; ++c) qa[c] = f32x4{0.f, 0.f, 0.f, 0.f};
  if constexpr (CW) waitv2<54>(wr[0][0], wr[0][1]); else waitv2<34>(wr[0][0], wr[0][1]);
  ldswrite16(wq_waddr, wr[0][0]);
  ldswrite16(wq_waddr + 1024, wr[0][1]);
#pragma unroll
  for (int c = 0; c < 10; ++c) {
    BARRIER();
    if constexpr (true) {
      if (c < 9) {
        if constexpr (CW) {
          switch (c) {
            case 0: waitv4<50>(wr[1][0], wr[1][1], xf[0], xf[1]); break;
            case 1: waitv4<46>(wr[2][0], wr[2][1], xf[2], xf[3]); break;
            case 2: waitv4<42>(wr[3][0], wr[3][1], xf[4], xf[5]); break;
            case 3: waitv4<38>(wr[4][0], wr[4][1], xf[6], xf[7]); break;
            case 4: waitv4<34>(wr[5][0], wr[5][1], xf[8], xf[9]); break;
            case 5: waitv4<30>(wr[6][0], wr[6][1], xf[10], xf[11]); break;
            case 6: waitv4<26>(wr[7][0], wr[7][1], xf[12], xf[13]); break;
            case 7: waitv4<22>(wr[8][0], wr[8][1], xf[14], xf[15]); break;
            case 8: waitv4<18>(wr[9][0], wr[9][1], xf[16], xf[17]); break;
          }
        } else {
          switch (c) {
            case 0: waitv2<32>(wr[1][0], wr[1][1]); break;
            case 1: waitv2<30>(wr[2][0], wr[2][1]); break;
            case 2: waitv2<28>(wr[3][0], wr[3][1]); break;
            case 3: waitv2<26>(wr[4][0], wr[4][1]); break;
            case 4: waitv2<24>(wr[5][0], wr[5][1]); break;
            case 5: waitv2<22>(wr[6][0], wr[6][1]); break;
            case 6: waitv2<20>(wr[7][0], wr[7][1]); break;
            case 7: waitv2<18>(wr[8][0], wr[8][1]); break;
            case 8: waitv2<16>(wr[9][0], wr[9][1]); break;
          }
        }
        const unsigned wa = wq_waddr + ((c + 1) & 1) * SLOT;
        ldswrite16(wa, wr[c < 9 ? c + 1 : 0][0]);
        ldswrite16(wa + 1024, wr[c < 9 ? c + 1 : 0][1]);
      } else {
        if constexpr (CW) waitv2<16>(xf[18], xf[19]);
        dma2(lds0 + vslot(0) * SLOT + wid * 2048, v_voff, vb);
      }
    }
    if constexpr (CW) {
      const unsigned sb = (8 + (c & 1)) * SLOT;
#pragma unroll
      for (int s = 0; s < 2; ++s)
#pragma unroll
        for (int d = 0; d < 8; ++d) {
          const unsigned a = (tr0 ^ (d << 5)) + sb + 8192 * s;
          qa[d] = mfma16(trpair(a, a + 1024), xf[2 * c + s], qa[d]);
        }
    }
  }
  bf16x8 qf[4];
  if constexpr (CW) {
#pragma unroll
    for (int w = 0; w < 4; ++w) {
      unsigned a0 = cvtpk(qa[2 * w][0], qa[2 * w][1]), a1 = cvtpk(qa[2 * w][2], qa[2 * w][3]);
      unsigned b0 = cvtpk(qa[2 * w + 1][0], qa[2 * w + 1][1]), b1 = cvtpk(qa[2 * w + 1][2], qa[2 * w + 1][3]);
      auto s0 = __builtin_amdgcn_permlane32_swap(a0, b0, false, false);
      auto s1 = __builtin_amdgcn_permlane32_swap(a1, b1, false, false);
      auto t0 = __builtin_amdgcn_permlane16_swap(s0[0], s0[1], false, false);
      auto t1 = __builtin_amdgcn_permlane16_swap(s1[0], s1[1], false, false);
      u32x4 v = {t0[0], t1[0], t0[1], t1[1]};
      qf[w] = __builtin_bit_cast(bf16x8, v);
    }
  }
  f32x4 sc[8][2][2];
#pragma unroll
  for (int t = 0; t < 8; ++t) {
    waitv<16>();
    BARRIER();
    if (t < 7) dma2(lds0 + vslot(t + 1) * SLOT + wid * 2048, v_voff, vb + (t + 1) * 64 * DH);
    if constexpr (CW) {
#pragma unroll
      for (int u = 0; u < 2; ++u)
#pragma unroll
        for (int e = 0; e < 2; ++e) {
          f32x4 acc = {0.f, 0.f, 0.f, 0.f};
#pragma unroll
          for (int s = 0; s < 4; ++s)
            acc = mfma16(ldsread16((kr0 ^ (s << 6)) + t * SLOT + 8192 * u + 1024 * e), qf[s], acc);
          sc[t][u][e] = acc;
        }
    }
  }
  bf16x8 pf[8][2];
  float linv = 0.f;
  if constexpr (CW) {
    float m = sc[0][0][0][0];
#pragma unroll
    for (int t = 0; t < 8; ++t)
#pragma unroll
      for (int u = 0; u < 2; ++u)
#pragma unroll
        for (int e = 0; e < 2; ++e)
#pragma unroll
          for (int j = 0; j < 4; ++j) m = fmaxf(m, sc[t][u][e][j]);
    m = fmaxf(m, __shfl_xor(m, 16));
    m = fmaxf(m, __shfl_xor(m, 32));
    constexpr float C = 0.08838834764831845f * 1.4426950408889634f;
    const float mc = -m * C;
    float l = 0.f;
#pragma unroll
    for (int t = 0; t < 8; ++t)
#pragma unroll
      for (int u = 0; u < 2; ++u) {
        float pv[8];
#pragma unroll
        for (int e = 0; e < 2; ++e)
#pragma unroll
          for (int j = 0; j < 4; ++j) {
            pv[4 * e + j] = __builtin_amdgcn_exp2f(fmaf(sc[t][u][e][j], C, mc));
            l += pv[4 * e + j];
          }
        u32x4 v = {cvtpk(pv[0], pv[1]), cvtpk(pv[2], pv[3]), cvtpk(pv[4], pv[5]), cvtpk(pv[6], pv[7])};
        pf[t][u] = __builtin_bit_cast(bf16x8, v);
      }
    l += __shfl_xor(l, 16);
    l += __shfl_xor(l, 32);
    linv = 1.0f / l;
  }
  f32x4 oa[8];
#pragma unroll
  for (int d = 0; d < 8; ++d) oa[d] = f32x4{0.f, 0.f, 0.f, 0.f};
#pragma unroll
  for (int t = 0; t < 8; ++t) {
    switch (t) {
      case 0: waitv<14>(); break;
      case 1: waitv<12>(); break;
      case 2: waitv<10>(); break;
      case 3: waitv<8>(); break;
      case 4: waitv<6>(); break;
      case 5: waitv<4>(); break;
      case 6: waitv<2>(); break;
      case 7: waitv<0>(); break;
    }
    BARRIER();
    if constexpr (CW) {
#pragma unroll
      for (int u = 0; u < 2; ++u)
#pragma unroll
        for (int d = 0; d < 8; ++d) {
          const unsigned a = (tr0 ^ (d << 5)) + vslot(t) * SLOT + 8192 * u;
          oa[d] = mfma16(trpair(a, a + 1024), pf[t][u], oa[d]);
        }
    }
  }
  if constexpr (CW) {
    uint16_t* op = ows + ((size_t)((b * 7 + wid) * 128 + h * 4) * 512) + lane * 8;
#pragma unroll
    for (int w = 0; w < 4; ++w) {
      u32x4 v = {cvtpk(oa[2 * w][0] * linv, oa[2 * w][1] * linv), cvtpk(oa[2 * w][2] * linv, oa[2 * w][3] * linv),
                 cvtpk(oa[2 * w + 1][0] * linv, oa[2 * w + 1][1] * linv), cvtpk(oa[2 * w + 1][2] * linv, oa[2 * w + 1][3] * linv)};
      *(u32x4*)(op + w * 512) = v;
    }
  }
}

__global__ __launch_bounds__(512, 2) void attn_kernel(const uint16_t* __restrict__ x, const uint16_t* __restrict__ kc,
                                                      const uint16_t* __restrict__ vc, const uint16_t* __restrict__ wq,
                                                      uint16_t* __restrict__ ows) {
  extern __shared__ __attribute__((aligned(16))) char smem[];
  const int lane = threadIdx.x & 63;
  const int wid = __builtin_amdgcn_readfirstlane(threadIdx.x >> 6);
  const int h = blockIdx.x & 31, b = blockIdx.x >> 5;
  const unsigned lds0 = (unsigned)(size_t)smem;
  if (wid < 7) attn_body<true>(x, kc, vc, wq, ows, lds0, wid, lane, b, h);
  else attn_body<false>(x, kc, vc, wq, ows, lds0, wid, lane, b, h);
}

constexpr int RING = 4, WSLOT = 5120;
__global__ __launch_bounds__(512, 2) void oproj_kernel(const uint16_t* __restrict__ ows, const uint16_t* __restrict__ wo,
                                                       uint16_t* __restrict__ out) {
  extern __shared__ __attribute__((aligned(16))) char smem[];
  const int lane = threadIdx.x & 63;
  const int wid = __builtin_amdgcn_readfirstlane(threadIdx.x >> 6);
  const int xcd = blockIdx.x & 7, idx = blockIdx.x >> 3;
  const int mp = (xcd >> 1) * 7 + idx % 7, nb = (xcd & 1) * 4 + idx / 7;
  const unsigned lds0 = (unsigned)(size_t)smem;
  const unsigned ring0 = lds0 + wid * (RING * WSLOT);
  const int r = lane & 15, g = lane >> 4, q4 = r >> 2, p = lane & 3;
  unsigned wvoff[5];
#pragma unroll
  for (int i = 0; i < 5; ++i) {
    const int id = 64 * i + lane, row = id / 10, cc = id % 10;
    wvoff[i] = (unsigned)((row * DM + 8 * cc) * 2);
  }
  const uint16_t* wo_s = wo + (size_t)(16 * wid * 32) * DM + 80 * nb;
  const uint16_t* o_s = ows + ((size_t)(2 * mp) * 128 + 16 * wid) * 512;
  const unsigned o_voff = (unsigned)lane * 16;
  const unsigned trb = ring0 + (unsigned)((4 * g + q4) * 160 + 8 * p);
  bf16x8 of[RING][2];
  f32x4 acc[5][2];
#pragma unroll
  for (int c = 0; c < 5; ++c) { acc[c][0] = f32x4{0.f, 0.f, 0.f, 0.f}; acc[c][1] = f32x4{0.f, 0.f, 0.f, 0.f}; }
  auto issue = [&](int ks, bf16x8& o0, bf16x8& o1) {
    gload<0>(o0, o_voff, o_s + ks * 512);
    gload<0>(o1, o_voff, o_s + ks * 512 + 128 * 512);
#pragma unroll
    for (int i = 0; i < 5; ++i) dma1(ring0 + (ks % RING) * WSLOT + 1024 * i, wvoff[i], wo_s + (size_t)ks * 32 * DM);
  };
#pragma unroll
  for (int ks = 0; ks < RING; ++ks) issue(ks, of[ks][0], of[ks][1]);
#pragma unroll
  for (int ks = 0; ks < 16; ++ks) {
    constexpr int DUMMY = 0; (void)DUMMY;
    if (ks <= 12) waitv2<21>(of[ks % RING][0], of[ks % RING][1]);
    else if (ks == 13) waitv2<14>(of[ks % RING][0], of[ks % RING][1]);
    else if (ks == 14) waitv2<7>(of[ks % RING][0], of[ks % RING][1]);
    else waitv2<0>(of[ks % RING][0], of[ks % RING][1]);
    bf16x8 af[5];
#pragma unroll
    for (int c = 0; c < 5; ++c) {
      const unsigned a = trb + (ks % RING) * WSLOT + 32 * c;
      af[c] = trpair(a, a + 2560);
    }
    bf16x8 b0 = of[ks % RING][0], b1 = of[ks % RING][1];
#pragma unroll
    for (int c = 0; c < 5; ++c) {
      acc[c][0] = mfma16(af[c], b0, acc[c][0]);
      acc[c][1] = mfma16(af[c], b1, acc[c][1]);
    }
    if (ks + RING < 16) {
      asm volatile("s_waitcnt lgkmcnt(0)" : "+v"(acc[4][1]) :: "memory");
      issue(ks + RING, of[ks % RING][0], of[ks % RING][1]);
    }
  }
  BARRIER();
#pragma unroll
  for (int c = 0; c < 5; ++c)
#pragma unroll
    for (int m = 0; m < 2; ++m) *(LDSP(f32x4))(size_t)(lds0 + wid * 10240 + (c * 2 + m) * 1024 + lane * 16) = acc[c][m];
  BARRIER();
  for (int it = threadIdx.x; it < 640; it += 512) {
    f32x4 s = *(LDSP(f32x4))(size_t)(lds0 + it * 16);
#pragma unroll
    for (int w = 1; w < 8; ++w) s += *(LDSP(f32x4))(size_t)(lds0 + w * 10240 + it * 16);
    const int tile = it >> 6, l = it & 63, c = tile >> 1, m = tile & 1;
    const int row = (2 * mp + m) * 16 + (l & 15), col = 80 * nb + 16 * c + 4 * (l >> 4);
    u32x2 v = {cvtpk(s[0], s[1]), cvtpk(s[2], s[3])};
    *(u32x2*)(out + (size_t)row * DM + col) = v;
  }
}

extern "C" void kernel_launch(void* const* d_in, const int* in_sizes, int n_in, void* d_out, int out_size, void* d_ws,
                              size_t ws_size, hipStream_t stream) {
  const uint16_t* x = (const uint16_t*)d_in[0];
  const uint16_t* kc = (const uint16_t*)d_in[1];
  const uint16_t* vc = (const uint16_t*)d_in[2];
  const uint16_t* wq = (const uint16_t*)d_in[3];
  const uint16_t* wo = (const uint16_t*)d_in[4];
  uint16_t* ows = (uint16_t*)d_ws;
  hipFuncSetAttribute((const void*)attn_kernel, hipFuncAttributeMaxDynamicSharedMemorySize, 10 * SLOT);
  hipFuncSetAttribute((const void*)oproj_kernel, hipFuncAttributeMaxDynamicSharedMemorySize, 8 * RING * WSLOT);
  attn_kernel<<<256, 512, 10 * SLOT, stream>>>(x, kc, vc, wq, ows);
  oproj_kernel<<<224, 512, 8 * RING * WSLOT, stream>>>(ows, wo, (uint16_t*)d_out);
}
```

```cpp
#include <hip/hip_runtime.h>
#include <hip/hip_bf16.h>
#include <stdint.h>

typedef __attribute__((ext_vector_type(8))) short bf16x8;
typedef __attribute__((ext_vector_type(4))) short s16x4;
typedef __attribute__((ext_vector_type(4))) float f32x4;
typedef __attribute__((ext_vector_type(4))) unsigned u32x4;
typedef __attribute__((ext_vector_type(2))) unsigned u32x2;
#define LDSP(T) __attribute__((address_space(3))) T*
#define DI __device__ __forceinline__

constexpr int NB = 8, SQ = 112, DM = 640, NH = 32, SK = 512, DH = 128;
constexpr int SLOT = 16384;

typedef __attribute__((ext_vector_type(2))) float f32x2;
typedef __attribute__((ext_vector_type(2))) __bf16 bf16x2;
DI unsigned cvtpk(float lo, float hi) {
  f32x2 v = {lo, hi};
  return __builtin_bit_cast(unsigned, __builtin_convertvector(v, bf16x2));
}
DI f32x4 mfma16(bf16x8 a, bf16x8 b, f32x4 c) { return __builtin_amdgcn_mfma_f32_16x16x32_bf16(a, b, c, 0, 0, 0); }
DI s16x4 trread(unsigned addr) { return __builtin_amdgcn_ds_read_tr16_b64_v4i16((LDSP(s16x4))(size_t)addr); }
DI bf16x8 trpair(unsigned a0, unsigned a1) {
  s16x4 lo = trread(a0), hi = trread(a1);
  return __builtin_shufflevector(lo, hi, 0, 1, 2, 3, 4, 5, 6, 7);
}
DI bf16x8 ldsread16(unsigned addr) { return *(LDSP(bf16x8))(size_t)addr; }
DI void ldswrite16(unsigned addr, bf16x8 v) { *(LDSP(bf16x8))(size_t)addr = v; }

template <int OFF> DI void gload(bf16x8& dst, unsigned voff, const void* sbase) {
  asm volatile("global_load_dwordx4 %0, %1, %2 offset:%3" : "=v"(dst) : "v"(voff), "s"(sbase), "i"(OFF) : "memory");
}
DI void dma2(unsigned m0val, unsigned voff, const void* sbase) {
  unsigned keep;
  asm volatile("s_mov_b32 %0, m0\n\ts_mov_b32 m0, %1\n\ts_nop 0\n\t"
               "global_load_lds_dwordx4 %2, %3 nt\n\tglobal_load_lds_dwordx4 %2, %3 offset:1024 nt\n\t"
               "s_mov_b32 m0, %0"
               : "=&s"(keep) : "s"(m0val), "v"(voff), "s"(sbase) : "memory");
}
DI void dma1(unsigned m0val, unsigned voff, const void* sbase) {
  unsigned keep;
  asm volatile("s_mov_b32 %0, m0\n\ts_mov_b32 m0, %1\n\ts_nop 0\n\t"
               "global_load_lds_dwordx4 %2, %3\n\t"
               "s_mov_b32 m0, %0"
               : "=&s"(keep) : "s"(m0val), "v"(voff), "s"(sbase) : "memory");
}
#define STAMP_DECL
#define STAMP(i)
#define RSTAMP(i)
#define STAMP_FLUSH
#define BARRIER() asm volatile("s_waitcnt lgkmcnt(0)\n\ts_barrier" ::: "memory")
template <int N> DI void waitv() { asm volatile("s_waitcnt vmcnt(%0)" ::"i"(N) : "memory"); }
template <int N> DI void waitv2(bf16x8& a, bf16x8& b) { asm volatile("s_waitcnt vmcnt(%2)" : "+v"(a), "+v"(b) : "i"(N) : "memory"); }
template <int N> DI void waitv4(bf16x8& a, bf16x8& b, bf16x8& c, bf16x8& d) {
  asm volatile("s_waitcnt vmcnt(%4)" : "+v"(a), "+v"(b), "+v"(c), "+v"(d) : "i"(N) : "memory");
}

template <int N> DI void waitv6(bf16x8& a, bf16x8& b, bf16x8& c, bf16x8& d, bf16x8& e, bf16x8& f) {
  asm volatile("s_waitcnt vmcnt(%6)" : "+v"(a), "+v"(b), "+v"(c), "+v"(d), "+v"(e), "+v"(f) : "i"(N) : "memory");
}
template <int N> DI void waitv3(bf16x8& a, bf16x8& b, bf16x8& c) {
  asm volatile("s_waitcnt vmcnt(%3)" : "+v"(a), "+v"(b), "+v"(c) : "i"(N) : "memory");
}
DI void dma4(unsigned m0val, unsigned voff, const void* sbase) {
  unsigned keep;
  asm volatile("s_mov_b32 %0, m0\n\ts_mov_b32 m0, %1\n\ts_nop 0\n\t"
               "global_load_lds_dwordx4 %2, %3\n\tglobal_load_lds_dwordx4 %2, %3 offset:1024\n\t"
               "global_load_lds_dwordx4 %2, %3 offset:2048\n\tglobal_load_lds_dwordx4 %2, %3 offset:3072\n\t"
               "s_mov_b32 m0, %0"
               : "=&s"(keep) : "s"(m0val), "v"(voff), "s"(sbase) : "memory");
}
#define OPAQUE(x) asm volatile("" : "+v"(x))
#define SCHED() __builtin_amdgcn_sched_barrier(0)

typedef __attribute__((ext_vector_type(16))) float f32x16;
DI f32x16 mfma32(bf16x8 a, bf16x8 b, f32x16 c) { return __builtin_amdgcn_mfma_f32_32x32x16_bf16(a, b, c, 0, 0, 0); }
template <int OFF> DI void gload_nt(bf16x8& dst, unsigned voff, const void* sbase) {
  asm volatile("global_load_dwordx4 %0, %1, %2 offset:%3 nt" : "=v"(dst) : "v"(voff), "s"(sbase), "i"(OFF) : "memory");
}
DI void dma1nt(unsigned m0val, unsigned voff, const void* sbase) {
  unsigned keep;
  asm volatile("s_mov_b32 %0, m0\n\ts_mov_b32 m0, %1\n\ts_nop 0\n\t"
               "global_load_lds_dwordx4 %2, %3 nt\n\t"
               "s_mov_b32 m0, %0"
               : "=&s"(keep) : "s"(m0val), "v"(voff), "s"(sbase) : "memory");
}
DI f32x4 ldsreadf(unsigned addr) { return *(LDSP(f32x4))(size_t)addr; }
DI void ldswritef(unsigned addr, f32x4 v) { *(LDSP(f32x4))(size_t)addr = v; }
DI f32x4 q4of(const f32x16& v, int i) { return f32x4{v[4 * i], v[4 * i + 1], v[4 * i + 2], v[4 * i + 3]}; }

__host__ __device__ constexpr int vslot(int t) { return t < 2 ? 8 + t : t - 2; }

DI void attn_body(const uint16_t* __restrict__ x, const uint16_t* __restrict__ kc, const uint16_t* __restrict__ vc,
                  const uint16_t* __restrict__ wq, uint16_t* __restrict__ ows, unsigned lds0, int wid, int lane, int b, int h) {
  const int r = lane & 15, g = lane >> 4;
  const int q = lane & 31, hh = lane >> 5;
  const int q4 = r >> 2, p = lane & 3, blk = g & 1;
  const int qg = wid & 3, kh = wid >> 2;
  STAMP_DECL RSTAMP(30); STAMP(0);
  const int rot = (b + 3 * (h >> 3)) % 10;
  const uint16_t* wq_s = wq + h * DH + (size_t)(8 * wid) * 4096;
  const uint16_t* x_s = x + (size_t)(b * SQ + 16 * (wid < 7 ? wid : 6)) * DM;
  const uint16_t* kb = kc + (size_t)(b * NH + h) * SK * DH + 8 * wid * DH;
  const uint16_t* vb = vc + (size_t)(b * NH + h) * SK * DH + 8 * wid * DH;
  auto unitof = [&](int hs) { int u = hs + rot; return u >= 10 ? u - 10 : u; };
  const unsigned w_voff = (unsigned)((g * 4096 + 8 * (r ^ (g << 2))) * 2);
  const unsigned v_voff = (unsigned)((g * DH + 8 * (r ^ (g << 2))) * 2);
  const unsigned k_voff0 = (unsigned)((g * DH + 8 * (r ^ (8 * (wid & 1) + g))) * 2);
  const unsigned k_voff1 = (unsigned)(((4 + g) * DH + 8 * (r ^ (8 * (wid & 1) + 4 + g))) * 2);
  const unsigned x_voff0 = (unsigned)(((lane >> 3) * DM + 8 * ((lane & 7) ^ (lane >> 4))) * 2);
  const unsigned x_voff1 = (unsigned)(((8 + (lane >> 3)) * DM + 8 * ((lane & 7) ^ (4 | (lane >> 4)))) * 2);
  auto dmaU1 = [&](int hs, int j) {
    const unsigned d = lds0 + 2 * (hs % 5) * SLOT + wid * 2048 + (j >> 1) * 1024;
    const int u = unitof(hs);
    if ((j & 1) == 0) dma1nt(d, w_voff, wq_s + (size_t)(64 * u + 4 * (j >> 1)) * 4096);
    else dma1(d + SLOT, (j >> 1) ? x_voff1 : x_voff0, x_s + 64 * u);
  };
  auto dmaU = [&](int hs) { dmaU1(hs, 0); dmaU1(hs, 1); dmaU1(hs, 2); dmaU1(hs, 3); };
  auto dmaK1 = [&](int t, int n) { dma1nt(lds0 + t * SLOT + wid * 2048 + n * 1024, n ? k_voff1 : k_voff0, kb + t * 64 * DH); };
  auto dmaV1 = [&](int t, int n) { dma1nt(lds0 + vslot(t) * SLOT + wid * 2048 + n * 1024, v_voff, vb + (t * 64 + 4 * n) * DH); };
  dmaU(0);
  asm volatile("s_barrier" ::: "memory");
  dmaU(1);
  STAMP(1);
  const unsigned tr0 = lds0 + (unsigned)((8 * hh + q4) * 256 + (q4 << 6) + 32 * blk + 8 * p);
  unsigned trb[4];
  const int xrow = 32 * qg + q;
  const unsigned xb = lds0 + SLOT + (unsigned)(xrow * 128 + ((hh ^ ((xrow >> 1) & 7)) << 4));

  f32x16 qa[2];
#pragma unroll
  for (int c = 0; c < 2; ++c)
#pragma unroll
    for (int i = 0; i < 16; ++i) qa[c][i] = 0.f;
  bf16x8 af[2][8], xf[2][4];
  auto dmaT = [&](int i) {
    if (i < 8) { dmaK1(i, 0); dmaK1(i, 1); } else if (i < 16) { dmaV1(i - 8, 0); dmaV1(i - 8, 1); }
  };
#pragma unroll
  for (int hs = 0; hs < 10; ++hs) {
    if (hs <= 8) waitv<4>(); else waitv<2>();
    if (hs == 0) STAMP(2);
    BARRIER();
    if (hs == 2) STAMP(3); if (hs == 6) STAMP(4);
#pragma unroll
    for (int c = 0; c < 2; ++c) { trb[c] = (tr0 ^ ((2 * kh + c) << 6)) + 2 * (hs % 5) * SLOT; OPAQUE(trb[c]); }
    const unsigned xa = xb + 2 * (hs % 5) * SLOT;
    SCHED();
#pragma unroll
    for (int d = 0; d < 8; ++d) {
      if (hs > 0) qa[d & 1] = mfma32(af[(hs - 1) & 1][d], xf[(hs - 1) & 1][d >> 1], qa[d & 1]);
      SCHED();
      if (d < 4) xf[hs & 1][d] = ldsread16(xa ^ (32 * d));
      if (d < 4) af[hs & 1][d] = trpair(trb[d & 1] + 4096 * (d >> 1), trb[d & 1] + 4096 * (d >> 1) + 1024);
      else if (d < 6) {
        af[hs & 1][2 * d - 4] = trpair(trb[0] + 4096 * (d - 2), trb[0] + 4096 * (d - 2) + 1024);
        af[hs & 1][2 * d - 3] = trpair(trb[1] + 4096 * (d - 2), trb[1] + 4096 * (d - 2) + 1024);
      }
      if ((d & 1) == 1 && hs + 2 < 10) dmaU1(hs + 2, d >> 1);
      if (d >= 6 && hs >= 8) dmaK1(hs - 8, d - 6);
      SCHED();
    }
  }
#pragma unroll
  for (int d = 0; d < 8; ++d) qa[d & 1] = mfma32(af[1][d], xf[1][d >> 1], qa[d & 1]);
  dmaT(2);
  bf16x8 qf[8];
  {
    const unsigned mine = lds0 + 6 * SLOT + wid * 4096 + lane * 16, other = lds0 + 6 * SLOT + (wid ^ 4) * 4096 + lane * 16;
    bf16x8 qm[4];
#pragma unroll
    for (int c = 0; c < 2; ++c)
#pragma unroll
      for (int s = 0; s < 2; ++s) {
        unsigned a0 = cvtpk(qa[c][8 * s], qa[c][8 * s + 1]), a1 = cvtpk(qa[c][8 * s + 2], qa[c][8 * s + 3]);
        unsigned b0 = cvtpk(qa[c][8 * s + 4], qa[c][8 * s + 5]), b1 = cvtpk(qa[c][8 * s + 6], qa[c][8 * s + 7]);
        auto s0 = __builtin_amdgcn_permlane32_swap(a0, b0, false, false);
        auto s1 = __builtin_amdgcn_permlane32_swap(a1, b1, false, false);
        u32x4 v = {s0[0], s1[0], s0[1], s1[1]};
        qm[2 * c + s] = __builtin_bit_cast(bf16x8, v);
        ldswrite16(mine + (2 * c + s) * 1024, qm[2 * c + s]);
      }
    BARRIER();
    dmaT(3);
#pragma unroll
    for (int i = 0; i < 4; ++i) {
      const bf16x8 o = ldsread16(other + i * 1024);
      qf[i] = kh ? o : qm[i];
      qf[4 + i] = kh ? qm[i] : o;
    }
  }
  STAMP(5);
  constexpr float C = 0.08838834764831845f * 1.4426950408889634f;
  constexpr float THR = 8.0f / C;
  const int key = (q & 19) | ((q & 4) << 1) | ((q & 8) >> 1);
  const unsigned kr0 = lds0 + kh * 8192 + (unsigned)(key * 256 + ((hh ^ (key & 15)) << 4));
  unsigned krb[8];
  bf16x8 kf[2][8];
  f32x16 sc[2], scn;
  bf16x8 pf[8][2];
  float alpha[8];
  unsigned resc = 0;
  float mref = 0.f, mc = 0.f;
  f32x2 ls = {0.f, 0.f};
  auto sm_max = [&](int t) {
    const f32x16& v = sc[t & 1];
    float m0 = fmaxf(fmaxf(v[0], v[1]), v[2]), m1 = fmaxf(fmaxf(v[3], v[4]), v[5]);
    m0 = fmaxf(fmaxf(m0, v[6]), v[7]); m1 = fmaxf(fmaxf(m1, v[8]), v[9]);
    m0 = fmaxf(fmaxf(m0, v[10]), v[11]); m1 = fmaxf(fmaxf(m1, v[12]), v[13]);
    m0 = fmaxf(fmaxf(m0, v[14]), v[15]);
    m0 = fmaxf(m0, m1);
    auto sw = __builtin_amdgcn_permlane32_swap(__float_as_uint(m0), __float_as_uint(m0), false, false);
    m0 = fmaxf(__uint_as_float(sw[0]), __uint_as_float(sw[1]));
    alpha[t] = 1.f;
    if (t == 0) { mref = m0; mc = -mref * C; }
    else if (__builtin_expect(__any(m0 - mref > THR), 0)) {
      const float mn = fmaxf(mref, m0);
      alpha[t] = __builtin_amdgcn_exp2f((mref - mn) * C);
      ls *= alpha[t];
      mref = mn; mc = -mref * C;
      resc |= 1u << t;
    }
  };
  unsigned pw[8][8];
  auto sm_exp = [&](int t, int j) {
    float e[4];
#pragma unroll
    for (int i = 0; i < 4; ++i) e[i] = __builtin_amdgcn_exp2f(fmaf(sc[t & 1][4 * j + i], C, mc));
    ls += f32x2{e[0], e[1]};
    ls += f32x2{e[2], e[3]};
    pw[t][2 * j] = cvtpk(e[0], e[1]);
    pw[t][2 * j + 1] = cvtpk(e[2], e[3]);
  };
#pragma unroll
  for (int t = 0; t < 10; ++t) {
    if (t < 8) {
      waitv<6>();
      BARRIER();
      STAMP(6 + t);
      if ((t & 3) == 0) {
#pragma unroll
        for (int s = 0; s < 8; ++s) { krb[s] = (kr0 ^ (s << 5)) + t * SLOT; OPAQUE(krb[s]); }
      }
    }
    if (t >= 2) sm_max(t - 2);
    SCHED();
#pragma unroll
    for (int s = 0; s < 8; ++s) {
      if (t >= 1 && t <= 8) {
        if (s == 0) {
          f32x16 z;
#pragma unroll
          for (int i = 0; i < 16; ++i) z[i] = 0.f;
          scn = mfma32(kf[(t - 1) & 1][0], qf[0], z);
        } else scn = mfma32(kf[(t - 1) & 1][s], qf[s], scn);
      }
      SCHED();
      if (t < 8 && s < 4) {
        kf[t & 1][2 * s] = ldsread16(krb[2 * s] + (t & 3) * SLOT);
        kf[t & 1][2 * s + 1] = ldsread16(krb[2 * s + 1] + (t & 3) * SLOT);
      }
      if (t >= 2 && (s & 1)) sm_exp(t - 2, s >> 1);
      SCHED();
    }
    if (t >= 1 && t <= 8) sc[(t - 1) & 1] = scn;
    if (t < 8) dmaT(4 + t);
  }
#pragma unroll
  for (int t = 0; t < 8; ++t)
#pragma unroll
    for (int s = 0; s < 2; ++s) {
      u32x4 v = {pw[t][4 * s], pw[t][4 * s + 1], pw[t][4 * s + 2], pw[t][4 * s + 3]};
      pf[t][s] = __builtin_bit_cast(bf16x8, v);
    }
  STAMP(14);
  f32x16 oa[4];
#pragma unroll
  for (int c = 0; c < 4; ++c)
#pragma unroll
    for (int i = 0; i < 16; ++i) oa[c][i] = 0.f;
  const int cs = 2 * (kh ^ 1);
#pragma unroll
  for (int t = 0; t < 8; ++t) {
    switch (t) {
      case 0: case 1: case 2: case 3: case 4: waitv<6>(); break;
      case 5: waitv<4>(); break;
      case 6: waitv<2>(); break;
      case 7: waitv<0>(); break;
    }
    BARRIER();
    STAMP(16 + t);
    if (t == 0 || t == 2 || t == 6) {
#pragma unroll
      for (int c = 0; c < 4; ++c) { trb[c] = (tr0 ^ ((c ^ cs) << 6)) + kh * 8192 + (t == 0 ? 8 * SLOT : t == 2 ? 0 : 4 * SLOT); OPAQUE(trb[c]); }
    }
    const unsigned so = (t < 2 ? t : t < 6 ? t - 2 : t - 6) * SLOT;
    if (t > 1) {
      if (__builtin_expect((resc >> (t - 1)) & 1, 0)) {
#pragma unroll
        for (int c = 0; c < 4; ++c) oa[c] *= alpha[t - 1];
      }
    }
    SCHED();
#pragma unroll
    for (int i = 0; i < 8; ++i) {
      if (t > 0) oa[i >> 1] = mfma32(af[(t - 1) & 1][i], pf[t - 1][i & 1], oa[i >> 1]);
      SCHED();
      if (i < 4) af[t & 1][i] = trpair(trb[i >> 1] + so + 4096 * (i & 1), trb[i >> 1] + so + 4096 * (i & 1) + 1024);
      else if (i < 6) {
        af[t & 1][2 * i - 4] = trpair(trb[i - 2] + so, trb[i - 2] + so + 1024);
        af[t & 1][2 * i - 3] = trpair(trb[i - 2] + so + 4096, trb[i - 2] + so + 4096 + 1024);
      }
      SCHED();
    }
    dmaT(12 + t);
  }
  if (__builtin_expect((resc >> 7) & 1, 0)) {
#pragma unroll
    for (int c = 0; c < 4; ++c) oa[c] *= alpha[7];
  }
#pragma unroll
  for (int i = 0; i < 4; ++i) oa[i >> 1] = mfma32(af[1][i], pf[7][i & 1], oa[i >> 1]);
  STAMP(24);
  {
    float l = ls[0] + ls[1];
    {
      auto sw = __builtin_amdgcn_permlane32_swap(__float_as_uint(l), __float_as_uint(l), false, false);
      l = __uint_as_float(sw[0]) + __uint_as_float(sw[1]);
    }
    const unsigned mine = lds0 + 6 * SLOT + wid * 8192 + lane * 16, other = lds0 + 6 * SLOT + (wid ^ 4) * 8192 + lane * 16;
    const unsigned mlm = lds0 + wid * 256 + q * 8, mlo = lds0 + (wid ^ 4) * 256 + q * 8;
    *(LDSP(f32x2))(size_t)mlm = f32x2{mref, l};
#pragma unroll
    for (int i = 0; i < 4; ++i) oa[2 + (i >> 1)] = mfma32(af[1][4 + i], pf[7][i & 1], oa[2 + (i >> 1)]);
#pragma unroll
    for (int c = 0; c < 2; ++c)
#pragma unroll
      for (int i = 0; i < 4; ++i) ldswritef(mine + (4 * c + i) * 1024, q4of(oa[c], i));
    BARRIER();
    const f32x2 ml = *(LDSP(f32x2))(size_t)mlo;
    const float m = fmaxf(mref, ml[0]);
    const float f0 = __builtin_amdgcn_exp2f((mref - m) * C), f1 = __builtin_amdgcn_exp2f((ml[0] - m) * C);
    const float linv = 1.0f / (l * f0 + ml[1] * f1);
    const float s0 = f0 * linv, s1 = f1 * linv;
    uint16_t* op = ows + ((size_t)((b * 7 + 2 * qg + (q >> 4)) * 128 + h * 4 + 2 * kh) * 512) + (hh * 16 + (q & 15)) * 8;
    if (xrow < SQ) {
#pragma unroll
      for (int c = 0; c < 2; ++c)
#pragma unroll
        for (int hf = 0; hf < 2; ++hf) {
          const f32x4 oA = q4of(oa[2 + c], 2 * hf) * s0 + ldsreadf(other + (4 * c + 2 * hf) * 1024) * s1;
          const f32x4 oB = q4of(oa[2 + c], 2 * hf + 1) * s0 + ldsreadf(other + (4 * c + 2 * hf + 1) * 1024) * s1;
          u32x4 v = {cvtpk(oA[0], oA[1]), cvtpk(oA[2], oA[3]), cvtpk(oB[0], oB[1]), cvtpk(oB[2], oB[3])};
          *(u32x4*)(op + c * 512 + hf * 256) = v;
        }
    }
  }
  STAMP(25); RSTAMP(31);
  STAMP_FLUSH;
}

__global__ __launch_bounds__(512, 2) void attn_kernel(const uint16_t* __restrict__ x, const uint16_t* __restrict__ kc,
                                                      const uint16_t* __restrict__ vc, const uint16_t* __restrict__ wq,
                                                      uint16_t* __restrict__ ows) {
  extern __shared__ __attribute__((aligned(16))) char smem[];
  const int lane = threadIdx.x & 63;
  const int wid = __builtin_amdgcn_readfirstlane(threadIdx.x >> 6);
  const int h = blockIdx.x & 31, b = blockIdx.x >> 5;
  const unsigned lds0 = (unsigned)(size_t)smem;
  attn_body(x, kc, vc, wq, ows, lds0, wid, lane, b, h);
  if (threadIdx.x == 0) *(unsigned*)((char*)ows + ((size_t)8 << 20) + (size_t)blockIdx.x * 128) = 0u;
}

constexpr int KSLOT = 13312;
constexpr size_t WS_CNT = (size_t)8 << 20;
constexpr size_t WS_X = (size_t)9 << 20;
template <int NM>
DI void oproj_body(const uint16_t* __restrict__ ows, const uint16_t* __restrict__ wo, uint16_t* __restrict__ out, char* __restrict__ ws,
                   unsigned lds0, int wid, int lane) {
  const int xcd = blockIdx.x & 7, idx = blockIdx.x >> 3;
  const int kq = xcd >> 1, mh = xcd & 1, mgi = idx / 5, nb = idx % 5;
  const int mt0 = 28 * mh + (mgi < 4 ? 5 * mgi : 20 + 4 * (mgi - 4)), mcnt = mgi < 4 ? 5 : 4;
  const int r = lane & 15, g = lane >> 4, q4 = r >> 2, p = lane & 3;
  const int rot = (mgi + nb) & 7;
  const unsigned o_voff = (unsigned)lane * 16;
  if constexpr (NM == 0) {
    const int lj = wid - 8;
    const unsigned w_voff0 = (unsigned)((g * DM + 8 * (r ^ (g << 1))) * 2), w_voff1 = (unsigned)((g * DM + 8 * (r ^ ((4 | g) << 1))) * 2);
    const uint16_t* wo_s = wo + (size_t)(1024 * kq + 32 * lj) * DM + 128 * nb;
    const uint16_t* o_s = ows + ((size_t)mt0 * 128 + 32 * kq + lj) * 512;
    auto issue = [&](int u) {
      const int uu = (u + rot) & 7;
      const unsigned d = lds0 + ((u % 3) * 4 + lj) * KSLOT;
#pragma unroll
      for (int i = 0; i < 8; ++i) {
        dma1(d + i * 1024, (i & 1) ? w_voff1 : w_voff0, wo_s + (size_t)(128 * uu + 4 * i) * DM);
        if (i < 5) dma1(d + 8192 + i * 1024, o_voff, o_s + ((size_t)(i < mcnt ? i : mcnt - 1) * 128 + 4 * uu) * 512);
      }
    };
    issue(0); issue(1);
#pragma unroll
    for (int u = 0; u < 8; ++u) {
      if (u < 7) waitv<13>(); else waitv<0>();
      BARRIER();
      if (u + 2 < 8) issue(u + 2);
    }
    return;
  } else {
  constexpr int M0 = NM == 3 ? 0 : 3;
  const int np = wid & 3;
  const int trow = 16 * (g >> 1) + 4 * (g & 1) + q4;
  const unsigned tra = lds0 + (unsigned)(trow * 256 + (((2 * np) ^ (trow & 7)) << 5) + 8 * p);
  const unsigned ofa = lds0 + 8192 + M0 * 1024 + lane * 16;
  f32x4 acc[2][NM];
#pragma unroll
  for (int c = 0; c < 2; ++c)
#pragma unroll
    for (int m = 0; m < NM; ++m) acc[c][m] = f32x4{0.f, 0.f, 0.f, 0.f};
  bf16x8 af[2][2], bf[2][NM];
#pragma unroll
  for (int u = 0; u < 8; ++u) {
    BARRIER();
    const unsigned ub = (u % 3) * 4 * KSLOT;
    unsigned t0 = tra + ub, t1 = (tra ^ 32) + ub, ob = ofa + ub;
    OPAQUE(t0); OPAQUE(t1); OPAQUE(ob);
    SCHED();
#pragma unroll
    for (int j = 0; j < 4; ++j) {
      af[j & 1][0] = trpair(t0 + j * KSLOT, t0 + j * KSLOT + 2048);
      af[j & 1][1] = trpair(t1 + j * KSLOT, t1 + j * KSLOT + 2048);
#pragma unroll
      for (int m = 0; m < NM; ++m) bf[j & 1][m] = ldsread16(ob + j * KSLOT + m * 1024);
      SCHED();
      if (u > 0 || j > 0) {
#pragma unroll
        for (int m = 0; m < NM; ++m) {
          acc[0][m] = mfma16(af[(j + 1) & 1][0], bf[(j + 1) & 1][m], acc[0][m]);
          acc[1][m] = mfma16(af[(j + 1) & 1][1], bf[(j + 1) & 1][m], acc[1][m]);
        }
      }
      SCHED();
    }
  }
#pragma unroll
  for (int m = 0; m < NM; ++m) {
    acc[0][m] = mfma16(af[1][0], bf[1][m], acc[0][m]);
    acc[1][m] = mfma16(af[1][1], bf[1][m], acc[1][m]);
  }
  const int dest = (idx << 3) | (np << 1) | mh;
  if (np != kq) {
    char* xp = ws + WS_X + ((size_t)(dest * 4 + kq) * 10 + 2 * M0) * 1024;
#pragma unroll
    for (int i = 0; i < 2 * NM; ++i)
      asm volatile("global_store_dwordx4 %0, %1, %2 offset:0 sc1\n\ts_nop 1" :: "v"(o_voff + (unsigned)(i * 1024)), "v"(acc[i & 1][i >> 1]), "s"(xp) : "memory");
    asm volatile("s_waitcnt vmcnt(0)" ::: "memory");
    if (lane == 0) __hip_atomic_fetch_add((unsigned*)(ws + WS_CNT + (size_t)dest * 128), 1u, __ATOMIC_RELAXED, __HIP_MEMORY_SCOPE_AGENT);
    return;
  }
  {
    unsigned* cnt = (unsigned*)(ws + WS_CNT + (size_t)blockIdx.x * 128);
    for (int spin = 0; spin < (1 << 22); ++spin) {
      if (__hip_atomic_load(cnt, __ATOMIC_RELAXED, __HIP_MEMORY_SCOPE_AGENT) >= 6u) break;
      __builtin_amdgcn_s_sleep(1);
    }
    asm volatile("s_barrier" ::: "memory");
    const char* xp = ws + WS_X + ((size_t)(blockIdx.x * 4) * 10 + 2 * M0) * 1024;
    f32x4 v[3][6];
#pragma unroll
    for (int s = 0; s < 3; ++s)
#pragma unroll
      for (int i = 0; i < 6; ++i) {
        if (i < 2 * NM)
          asm volatile("global_load_dwordx4 %0, %1, %2 offset:0 sc1" : "=v"(v[s][i]) : "v"(o_voff + (unsigned)(i * 1024)), "s"(xp + (size_t)((kq + s + 1) & 3) * 10240) : "memory");
        else v[s][i] = f32x4{0.f, 0.f, 0.f, 0.f};
      }
    asm volatile("s_waitcnt vmcnt(0)" : "+v"(v[0][0]), "+v"(v[0][1]), "+v"(v[0][2]), "+v"(v[0][3]), "+v"(v[0][4]), "+v"(v[0][5]),
                 "+v"(v[1][0]), "+v"(v[1][1]), "+v"(v[1][2]), "+v"(v[1][3]), "+v"(v[1][4]), "+v"(v[1][5]),
                 "+v"(v[2][0]), "+v"(v[2][1]), "+v"(v[2][2]), "+v"(v[2][3]), "+v"(v[2][4]), "+v"(v[2][5]) :: "memory");
#pragma unroll
    for (int i = 0; i < 2 * NM; ++i) {
      f32x4 t = acc[i & 1][i >> 1];
      t += v[0][i]; t += v[1][i]; t += v[2][i];
      if (M0 + (i >> 1) < mcnt) {
        u32x2 o = {cvtpk(t[0], t[1]), cvtpk(t[2], t[3])};
        *(u32x2*)(out + (size_t)((mt0 + M0 + (i >> 1)) * 16 + r) * DM + 128 * nb + 16 * (2 * np + (i & 1)) + 4 * g) = o;
      }
    }
  }
  }
}
__global__ __launch_bounds__(768, 3) void oproj_kernel(const uint16_t* __restrict__ ows, const uint16_t* __restrict__ wo,
                                                       uint16_t* __restrict__ out, char* __restrict__ ws) {
  extern __shared__ __attribute__((aligned(16))) char smem[];
  const int lane = threadIdx.x & 63;
  const int wid = __builtin_amdgcn_readfirstlane(threadIdx.x >> 6);
  const unsigned lds0 = (unsigned)(size_t)smem;
  if (wid < 4) oproj_body<3>(ows, wo, out, ws, lds0, wid, lane);
  else if (wid < 8) oproj_body<2>(ows, wo, out, ws, lds0, wid, lane);
  else oproj_body<0>(ows, wo, out, ws, lds0, wid, lane);
}

extern "C" void kernel_launch(void* const* d_in, const int* in_sizes, int n_in, void* d_out, int out_size, void* d_ws,
                              size_t ws_size, hipStream_t stream) {
  const uint16_t* x = (const uint16_t*)d_in[0];
  const uint16_t* kc = (const uint16_t*)d_in[1];
  const uint16_t* vc = (const uint16_t*)d_in[2];
  const uint16_t* wq = (const uint16_t*)d_in[3];
  const uint16_t* wo = (const uint16_t*)d_in[4];
  uint16_t* ows = (uint16_t*)d_ws;
  hipFuncSetAttribute((const void*)attn_kernel, hipFuncAttributeMaxDynamicSharedMemorySize, 10 * SLOT);
  hipFuncSetAttribute((const void*)oproj_kernel, hipFuncAttributeMaxDynamicSharedMemorySize, 12 * KSLOT);
  attn_kernel<<<256, 512, 10 * SLOT, stream>>>(x, kc, vc, wq, ows);
  oproj_kernel<<<240, 768, 12 * KSLOT, stream>>>(ows, wo, (uint16_t*)d_out, (char*)d_ws);
}
```

```cpp
#include <hip/hip_runtime.h>
#include <hip/hip_bf16.h>
#include <stdint.h>

typedef __attribute__((ext_vector_type(8))) short bf16x8;
typedef __attribute__((ext_vector_type(4))) short s16x4;
typedef __attribute__((ext_vector_type(4))) float f32x4;
typedef __attribute__((ext_vector_type(4))) unsigned u32x4;
typedef __attribute__((ext_vector_type(2))) unsigned u32x2;
#define LDSP(T) __attribute__((address_space(3))) T*
#define DI __device__ __forceinline__

constexpr int NB = 8, SQ = 112, DM = 640, NH = 32, SK = 512, DH = 128;
constexpr int SLOT = 16384;

typedef __attribute__((ext_vector_type(2))) float f32x2;
typedef __attribute__((ext_vector_type(2))) __bf16 bf16x2;
DI unsigned cvtpk(float lo, float hi) {
  f32x2 v = {lo, hi};
  return __builtin_bit_cast(unsigned, __builtin_convertvector(v, bf16x2));
}
DI f32x4 mfma16(bf16x8 a, bf16x8 b, f32x4 c) { return __builtin_amdgcn_mfma_f32_16x16x32_bf16(a, b, c, 0, 0, 0); }
DI s16x4 trread(unsigned addr) { return __builtin_amdgcn_ds_read_tr16_b64_v4i16((LDSP(s16x4))(size_t)addr); }
DI bf16x8 trpair(unsigned a0, unsigned a1) {
  s16x4 lo = trread(a0), hi = trread(a1);
  return __builtin_shufflevector(lo, hi, 0, 1, 2, 3, 4, 5, 6, 7);
}
DI bf16x8 ldsread16(unsigned addr) { return *(LDSP(bf16x8))(size_t)addr; }
DI void ldswrite16(unsigned addr, bf16x8 v) { *(LDSP(bf16x8))(size_t)addr = v; }

template <int OFF> DI void gload(bf16x8& dst, unsigned voff, const void* sbase) {
  asm volatile("global_load_dwordx4 %0, %1, %2 offset:%3" : "=v"(dst) : "v"(voff), "s"(sbase), "i"(OFF) : "memory");
}
DI void dma2(unsigned m0val, unsigned voff, const void* sbase) {
  unsigned keep;
  asm volatile("s_mov_b32 %0, m0\n\ts_mov_b32 m0, %1\n\ts_nop 0\n\t"
               "global_load_lds_dwordx4 %2, %3 nt\n\tglobal_load_lds_dwordx4 %2, %3 offset:1024 nt\n\t"
               "s_mov_b32 m0, %0"
               : "=&s"(keep) : "s"(m0val), "v"(voff), "s"(sbase) : "memory");
}
DI void dma1(unsigned m0val, unsigned voff, const void* sbase) {
  unsigned keep;
  asm volatile("s_mov_b32 %0, m0\n\ts_mov_b32 m0, %1\n\ts_nop 0\n\t"
               "global_load_lds_dwordx4 %2, %3\n\t"
               "s_mov_b32 m0, %0"
               : "=&s"(keep) : "s"(m0val), "v"(voff), "s"(sbase) : "memory");
}
#define STAMP_DECL
#define STAMP(i)
#define RSTAMP(i)
#define STAMP_FLUSH
#define BARRIER() asm volatile("s_waitcnt lgkmcnt(0)\n\ts_barrier" ::: "memory")
template <int N> DI void waitv() { asm volatile("s_waitcnt vmcnt(%0)" ::"i"(N) : "memory"); }
template <int N> DI void waitv2(bf16x8& a, bf16x8& b) { asm volatile("s_waitcnt vmcnt(%2)" : "+v"(a), "+v"(b) : "i"(N) : "memory"); }
template <int N> DI void waitv4(bf16x8& a, bf16x8& b, bf16x8& c, bf16x8& d) {
  asm volatile("s_waitcnt vmcnt(%4)" : "+v"(a), "+v"(b), "+v"(c), "+v"(d) : "i"(N) : "memory");
}

template <int N> DI void waitv6(bf16x8& a, bf16x8& b, bf16x8& c, bf16x8& d, bf16x8& e, bf16x8& f) {
  asm volatile("s_waitcnt vmcnt(%6)" : "+v"(a), "+v"(b), "+v"(c), "+v"(d), "+v"(e), "+v"(f) : "i"(N) : "memory");
}
template <int N> DI void waitv3(bf16x8& a, bf16x8& b, bf16x8& c) {
  asm volatile("s_waitcnt vmcnt(%3)" : "+v"(a), "+v"(b), "+v"(c) : "i"(N) : "memory");
}
DI void dma4(unsigned m0val, unsigned voff, const void* sbase) {
  unsigned keep;
  asm volatile("s_mov_b32 %0, m0\n\ts_mov_b32 m0, %1\n\ts_nop 0\n\t"
               "global_load_lds_dwordx4 %2, %3\n\tglobal_load_lds_dwordx4 %2, %3 offset:1024\n\t"
               "global_load_lds_dwordx4 %2, %3 offset:2048\n\tglobal_load_lds_dwordx4 %2, %3 offset:3072\n\t"
               "s_mov_b32 m0, %0"
               : "=&s"(keep) : "s"(m0val), "v"(voff), "s"(sbase) : "memory");
}
#define OPAQUE(x) asm volatile("" : "+v"(x))
#define SCHED() __builtin_amdgcn_sched_barrier(0)

typedef __attribute__((ext_vector_type(16))) float f32x16;
DI f32x16 mfma32(bf16x8 a, bf16x8 b, f32x16 c) { return __builtin_amdgcn_mfma_f32_32x32x16_bf16(a, b, c, 0, 0, 0); }
template <int OFF> DI void gload_nt(bf16x8& dst, unsigned voff, const void* sbase) {
  asm volatile("global_load_dwordx4 %0, %1, %2 offset:%3 nt" : "=v"(dst) : "v"(voff), "s"(sbase), "i"(OFF) : "memory");
}
DI void dma1nt(unsigned m0val, unsigned voff, const void* sbase) {
  unsigned keep;
  asm volatile("s_mov_b32 %0, m0\n\ts_mov_b32 m0, %1\n\ts_nop 0\n\t"
               "global_load_lds_dwordx4 %2, %3 nt\n\t"
               "s_mov_b32 m0, %0"
               : "=&s"(keep) : "s"(m0val), "v"(voff), "s"(sbase) : "memory");
}
DI f32x4 ldsreadf(unsigned addr) { return *(LDSP(f32x4))(size_t)addr; }
DI void ldswritef(unsigned addr, f32x4 v) { *(LDSP(f32x4))(size_t)addr = v; }
DI f32x4 q4of(const f32x16& v, int i) { return f32x4{v[4 * i], v[4 * i + 1], v[4 * i + 2], v[4 * i + 3]}; }

__host__ __device__ constexpr int vslot(int t) { return t < 2 ? 8 + t : t - 2; }

DI void attn_body(const uint16_t* __restrict__ x, const uint16_t* __restrict__ kc, const uint16_t* __restrict__ vc,
                  const uint16_t* __restrict__ wq, uint16_t* __restrict__ ows, unsigned lds0, int wid, int lane, int b, int h) {
  const int r = lane & 15, g = lane >> 4;
  const int q = lane & 31, hh = lane >> 5;
  const int q4 = r >> 2, p = lane & 3, blk = g & 1;
  const int qg = wid & 3, kh = wid >> 2;
  STAMP_DECL RSTAMP(30); STAMP(0);
  const int rot = (b + 3 * (h >> 3)) % 10;
  const uint16_t* wq_s = wq + h * DH + (size_t)(8 * wid) * 4096;
  const uint16_t* x_s = x + (size_t)(b * SQ + 16 * (wid < 7 ? wid : 6)) * DM;
  const uint16_t* kb = kc + (size_t)(b * NH + h) * SK * DH + 8 * wid * DH;
  const uint16_t* vb = vc + (size_t)(b * NH + h) * SK * DH + 8 * wid * DH;
  auto unitof = [&](int hs) { int u = hs + rot; return u >= 10 ? u - 10 : u; };
  const unsigned w_voff = (unsigned)((g * 4096 + 8 * (r ^ (g << 2))) * 2);
  const unsigned v_voff = (unsigned)((g * DH + 8 * (r ^ (g << 2))) * 2);
  const unsigned k_voff0 = (unsigned)((g * DH + 8 * (r ^ (8 * (wid & 1) + g))) * 2);
  const unsigned k_voff1 = (unsigned)(((4 + g) * DH + 8 * (r ^ (8 * (wid & 1) + 4 + g))) * 2);
  const unsigned x_voff0 = (unsigned)(((lane >> 3) * DM + 8 * ((lane & 7) ^ (lane >> 4))) * 2);
  const unsigned x_voff1 = (unsigned)(((8 + (lane >> 3)) * DM + 8 * ((lane & 7) ^ (4 | (lane >> 4)))) * 2);
  auto dmaU1 = [&](int hs, int j) {
    const unsigned d = lds0 + 2 * (hs % 5) * SLOT + wid * 2048 + (j >> 1) * 1024;
    const int u = unitof(hs);
    if ((j & 1) == 0) dma1nt(d, w_voff, wq_s + (size_t)(64 * u + 4 * (j >> 1)) * 4096);
    else dma1(d + SLOT, (j >> 1) ? x_voff1 : x_voff0, x_s + 64 * u);
  };
  auto dmaU = [&](int hs) { dmaU1(hs, 0); dmaU1(hs, 1); dmaU1(hs, 2); dmaU1(hs, 3); };
  auto dmaK1 = [&](int t, int n) { dma1nt(lds0 + t * SLOT + wid * 2048 + n * 1024, n ? k_voff1 : k_voff0, kb + t * 64 * DH); };
  auto dmaV1 = [&](int t, int n) { dma1nt(lds0 + vslot(t) * SLOT + wid * 2048 + n * 1024, v_voff, vb + (t * 64 + 4 * n) * DH); };
  dmaU(0);
  asm volatile("s_barrier" ::: "memory");
  dmaU(1);
  STAMP(1);
  const unsigned tr0 = lds0 + (unsigned)((8 * hh + q4) * 256 + (q4 << 6) + 32 * blk + 8 * p);
  unsigned trb[4];
  const int xrow = 32 * qg + q;
  const unsigned xb = lds0 + SLOT + (unsigned)(xrow * 128 + ((hh ^ ((xrow >> 1) & 7)) << 4));

  f32x16 qa[2];
#pragma unroll
  for (int c = 0; c < 2; ++c)
#pragma unroll
    for (int i = 0; i < 16; ++i) qa[c][i] = 0.f;
  bf16x8 af[2][8], xf[2][4];
  auto dmaT = [&](int i) {
    if (i < 8) { dmaK1(i, 0); dmaK1(i, 1); } else if (i < 16) { dmaV1(i - 8, 0); dmaV1(i - 8, 1); }
  };
#pragma unroll
  for (int hs = 0; hs < 10; ++hs) {
    if (hs <= 8) waitv<4>(); else waitv<2>();
    if (hs == 0) STAMP(2);
    BARRIER();
    if (hs == 2) STAMP(3); if (hs == 6) STAMP(4);
#pragma unroll
    for (int c = 0; c < 2; ++c) { trb[c] = (tr0 ^ ((2 * kh + c) << 6)) + 2 * (hs % 5) * SLOT; OPAQUE(trb[c]); }
    const unsigned xa = xb + 2 * (hs % 5) * SLOT;
    SCHED();
#pragma unroll
    for (int d = 0; d < 8; ++d) {
      if (hs > 0) qa[d & 1] = mfma32(af[(hs - 1) & 1][d], xf[(hs - 1) & 1][d >> 1], qa[d & 1]);
      SCHED();
      if (d < 4) xf[hs & 1][d] = ldsread16(xa ^ (32 * d));
      if (d < 4) af[hs & 1][d] = trpair(trb[d & 1] + 4096 * (d >> 1), trb[d & 1] + 4096 * (d >> 1) + 1024);
      else if (d < 6) {
        af[hs & 1][2 * d - 4] = trpair(trb[0] + 4096 * (d - 2), trb[0] + 4096 * (d - 2) + 1024);
        af[hs & 1][2 * d - 3] = trpair(trb[1] + 4096 * (d - 2), trb[1] + 4096 * (d - 2) + 1024);
      }
      if ((d & 1) == 1 && hs + 2 < 10) dmaU1(hs + 2, d >> 1);
      if (d < 2 && hs >= 8) dmaK1(hs - 8, d);
      SCHED();
    }
  }
#pragma unroll
  for (int d = 0; d < 8; ++d) qa[d & 1] = mfma32(af[1][d], xf[1][d >> 1], qa[d & 1]);
  dmaT(2);
  constexpr float C = 0.08838834764831845f * 1.4426950408889634f;
  qa[0] *= C; qa[1] *= C;
  bf16x8 qf[8];
  {
    const unsigned mine = lds0 + 6 * SLOT + wid * 4096 + lane * 16, other = lds0 + 6 * SLOT + (wid ^ 4) * 4096 + lane * 16;
    bf16x8 qm[4];
#pragma unroll
    for (int c = 0; c < 2; ++c)
#pragma unroll
      for (int s = 0; s < 2; ++s) {
        unsigned a0 = cvtpk(qa[c][8 * s], qa[c][8 * s + 1]), a1 = cvtpk(qa[c][8 * s + 2], qa[c][8 * s + 3]);
        unsigned b0 = cvtpk(qa[c][8 * s + 4], qa[c][8 * s + 5]), b1 = cvtpk(qa[c][8 * s + 6], qa[c][8 * s + 7]);
        auto s0 = __builtin_amdgcn_permlane32_swap(a0, b0, false, false);
        auto s1 = __builtin_amdgcn_permlane32_swap(a1, b1, false, false);
        u32x4 v = {s0[0], s1[0], s0[1], s1[1]};
        qm[2 * c + s] = __builtin_bit_cast(bf16x8, v);
        ldswrite16(mine + (2 * c + s) * 1024, qm[2 * c + s]);
      }
    BARRIER();
    dmaT(3);
#pragma unroll
    for (int i = 0; i < 4; ++i) {
      const bf16x8 o = ldsread16(other + i * 1024);
      qf[i] = kh ? o : qm[i];
      qf[4 + i] = kh ? qm[i] : o;
    }
  }
  STAMP(5);
  constexpr float THR = 8.0f;
  const int key = (q & 19) | ((q & 4) << 1) | ((q & 8) >> 1);
  const unsigned kr0 = lds0 + kh * 8192 + (unsigned)(key * 256 + ((hh ^ (key & 15)) << 4));
  unsigned krb[8];
  bf16x8 kf[2][8];
  f32x16 sc[2], scn, minit;
#pragma unroll
  for (int i = 0; i < 16; ++i) minit[i] = 0.f;
  bf16x8 pf[8][2];
  float alpha[8];
  unsigned resc = 0;
  float mref = 0.f, off = 0.f;
  bool offf = false;
  auto sm_max = [&](int t) {
    f32x16& v = sc[t & 1];
    float m0 = fmaxf(fmaxf(v[0], v[1]), v[2]), m1 = fmaxf(fmaxf(v[3], v[4]), v[5]);
    m0 = fmaxf(fmaxf(m0, v[6]), v[7]); m1 = fmaxf(fmaxf(m1, v[8]), v[9]);
    m0 = fmaxf(fmaxf(m0, v[10]), v[11]); m1 = fmaxf(fmaxf(m1, v[12]), v[13]);
    m0 = fmaxf(fmaxf(m0, v[14]), v[15]);
    m0 = fmaxf(m0, m1);
    auto sw = __builtin_amdgcn_permlane32_swap(__float_as_uint(m0), __float_as_uint(m0), false, false);
    m0 = fmaxf(__uint_as_float(sw[0]), __uint_as_float(sw[1]));
    alpha[t] = 1.f;
    m0 -= off;
    if (t <= 1 || __builtin_expect(offf || __any(m0 > THR), 0)) {
      const bool upd = t == 0 || __any(m0 > THR);
      const float dl = t == 0 ? m0 : (upd ? fmaxf(m0, 0.f) : 0.f);
      if (t > 0 && upd) { alpha[t] = __builtin_amdgcn_exp2f(-dl); resc |= 1u << t; }
      mref += dl;
      v -= off + dl;
      off = dl; offf = upd;
#pragma unroll
      for (int i = 0; i < 16; ++i) minit[i] = -mref;
    }
  };
  unsigned pw[8][8];
  float sh[8][8];
  auto sm_exp = [&](int t, int j) {
    float e[4];
#pragma unroll
    for (int i = 0; i < 4; ++i) e[i] = __builtin_amdgcn_exp2f(sc[t & 1][4 * j + i]);
    pw[t][2 * j] = cvtpk(e[0], e[1]);
    pw[t][2 * j + 1] = cvtpk(e[2], e[3]);
    asm volatile("" : "+v"(pw[t][2 * j]), "+v"(pw[t][2 * j + 1]));
  };
#pragma unroll
  for (int t = 0; t < 10; ++t) {
    if (t < 8) {
      waitv<6>();
      BARRIER();
      STAMP(6 + t);
      if ((t & 3) == 0) {
#pragma unroll
        for (int s = 0; s < 8; ++s) { krb[s] = (kr0 ^ (s << 5)) + t * SLOT; OPAQUE(krb[s]); }
      }
    }
    SCHED();
#pragma unroll
    for (int s = 0; s < 8; ++s) {
      if (t >= 1 && t <= 8) {
        if (s == 0) scn = mfma32(kf[(t - 1) & 1][0], qf[0], minit);
        else scn = mfma32(kf[(t - 1) & 1][s], qf[s], scn);
      }
      SCHED();
      if (t < 8 && s < 4) {
        kf[t & 1][2 * s] = ldsread16(krb[2 * s] + (t & 3) * SLOT);
        kf[t & 1][2 * s + 1] = ldsread16(krb[2 * s + 1] + (t & 3) * SLOT);
      }
      if (t >= 2 && s == 1) sm_max(t - 2);
      if (t >= 2 && (s == 3 || s == 5)) sm_exp(t - 2, (s - 3) >> 1);
      if (t >= 2 && s == 2) {
#pragma unroll
        for (int i = 0; i < 8; ++i) sh[t - 2][i] = sc[t & 1][8 + i];
      }
      SCHED();
    }
    if (t >= 1 && t <= 8) sc[(t - 1) & 1] = scn;
    if (t < 8) dmaT(4 + t);
  }
#pragma unroll
  for (int t = 0; t < 8; ++t) {
    u32x4 v = {pw[t][0], pw[t][1], pw[t][2], pw[t][3]};
    pf[t][0] = __builtin_bit_cast(bf16x8, v);
  }
  auto sm_exp2 = [&](int t, int j) {
    float e[4];
#pragma unroll
    for (int i = 0; i < 4; ++i) e[i] = __builtin_amdgcn_exp2f(sh[t][4 * j + i]);
    pw[t][4 + 2 * j] = cvtpk(e[0], e[1]);
    pw[t][5 + 2 * j] = cvtpk(e[2], e[3]);
    asm volatile("" : "+v"(pw[t][4 + 2 * j]), "+v"(pw[t][5 + 2 * j]));
    if (j == 1) {
      u32x4 v = {pw[t][4], pw[t][5], pw[t][6], pw[t][7]};
      pf[t][1] = __builtin_bit_cast(bf16x8, v);
    }
  };
  STAMP(14);
  f32x16 oa[4];
#pragma unroll
  for (int c = 0; c < 4; ++c)
#pragma unroll
    for (int i = 0; i < 16; ++i) oa[c][i] = 0.f;
  f32x16 la;
#pragma unroll
  for (int i = 0; i < 16; ++i) la[i] = 0.f;
  const bf16x8 ones = {0x3F80, 0x3F80, 0x3F80, 0x3F80, 0x3F80, 0x3F80, 0x3F80, 0x3F80};
  const int cs = 2 * (kh ^ 1);
#pragma unroll
  for (int t = 0; t < 8; ++t) {
    switch (t) {
      case 0: case 1: case 2: case 3: case 4: waitv<6>(); break;
      case 5: waitv<4>(); break;
      case 6: waitv<2>(); break;
      case 7: waitv<0>(); break;
    }
    BARRIER();
    STAMP(16 + t);
    if (t == 0 || t == 2 || t == 6) {
#pragma unroll
      for (int c = 0; c < 4; ++c) { trb[c] = (tr0 ^ ((c ^ cs) << 6)) + kh * 8192 + (t == 0 ? 8 * SLOT : t == 2 ? 0 : 4 * SLOT); OPAQUE(trb[c]); }
    }
    const unsigned so = (t < 2 ? t : t < 6 ? t - 2 : t - 6) * SLOT;
    if (t > 1) {
      if (__builtin_expect((resc >> (t - 1)) & 1, 0)) {
#pragma unroll
        for (int c = 0; c < 4; ++c) oa[c] *= alpha[t - 1];
        la *= alpha[t - 1];
      }
    }
    SCHED();
#pragma unroll
    for (int i = 0; i < 8; ++i) {
      if (t > 0) oa[i >> 1] = mfma32(af[(t - 1) & 1][i], pf[t - 1][i & 1], oa[i >> 1]);
      SCHED();
      if (i < 4) af[t & 1][i] = trpair(trb[i >> 1] + so + 4096 * (i & 1), trb[i >> 1] + so + 4096 * (i & 1) + 1024);
      else if (i < 6) {
        af[t & 1][2 * i - 4] = trpair(trb[i - 2] + so, trb[i - 2] + so + 1024);
        af[t & 1][2 * i - 3] = trpair(trb[i - 2] + so + 4096, trb[i - 2] + so + 4096 + 1024);
      }
      if (i == 2 || i == 6) sm_exp2(t, i >> 2);
      SCHED();
    }
    if (t > 0) { la = mfma32(ones, pf[t - 1][0], la); la = mfma32(ones, pf[t - 1][1], la); }
    SCHED();
    dmaT(12 + t);
  }
  if (__builtin_expect((resc >> 7) & 1, 0)) {
#pragma unroll
    for (int c = 0; c < 4; ++c) oa[c] *= alpha[7];
    la *= alpha[7];
  }
  la = mfma32(ones, pf[7][0], la); la = mfma32(ones, pf[7][1], la);
#pragma unroll
  for (int i = 0; i < 4; ++i) oa[i >> 1] = mfma32(af[1][i], pf[7][i & 1], oa[i >> 1]);
  STAMP(24);
  {
    const float l = la[0];
    const unsigned mine = lds0 + 6 * SLOT + wid * 4096 + lane * 16, other = lds0 + 6 * SLOT + (wid ^ 4) * 4096 + lane * 16;
    const unsigned mlm = lds0 + wid * 256 + q * 8, mlo = lds0 + (wid ^ 4) * 256 + q * 8;
    *(LDSP(f32x2))(size_t)mlm = f32x2{mref, l};
#pragma unroll
    for (int i = 0; i < 4; ++i) oa[2 + (i >> 1)] = mfma32(af[1][4 + i], pf[7][i & 1], oa[2 + (i >> 1)]);
#pragma unroll
    for (int c = 0; c < 2; ++c)
#pragma unroll
      for (int hf = 0; hf < 2; ++hf) {
        u32x4 v = {cvtpk(oa[c][8 * hf], oa[c][8 * hf + 1]), cvtpk(oa[c][8 * hf + 2], oa[c][8 * hf + 3]),
                   cvtpk(oa[c][8 * hf + 4], oa[c][8 * hf + 5]), cvtpk(oa[c][8 * hf + 6], oa[c][8 * hf + 7])};
        ldswrite16(mine + (2 * c + hf) * 1024, __builtin_bit_cast(bf16x8, v));
      }
    BARRIER();
    const f32x2 ml = *(LDSP(f32x2))(size_t)mlo;
    u32x4 pv[4];
#pragma unroll
    for (int i = 0; i < 4; ++i) pv[i] = __builtin_bit_cast(u32x4, ldsread16(other + i * 1024));
    const float m = fmaxf(mref, ml[0]);
    const float f0 = __builtin_amdgcn_exp2f(mref - m), f1 = __builtin_amdgcn_exp2f(ml[0] - m);
    const float linv = __builtin_amdgcn_rcpf(l * f0 + ml[1] * f1);
    const float s0 = f0 * linv, s1 = f1 * linv;
    uint16_t* op = ows + ((size_t)((b * 7 + 2 * qg + (q >> 4)) * 128 + h * 4 + 2 * kh) * 512) + (hh * 16 + (q & 15)) * 8;
    if (xrow < SQ) {
#pragma unroll
      for (int c = 0; c < 2; ++c)
#pragma unroll
        for (int hf = 0; hf < 2; ++hf) {
          u32x4 v;
#pragma unroll
          for (int i = 0; i < 4; ++i) {
            const unsigned w = pv[2 * c + hf][i];
            const float lo = fmaf(oa[2 + c][8 * hf + 2 * i], s0, __uint_as_float(w << 16) * s1);
            const float hi = fmaf(oa[2 + c][8 * hf + 2 * i + 1], s0, __uint_as_float(w & 0xffff0000u) * s1);
            v[i] = cvtpk(lo, hi);
          }
          *(u32x4*)(op + c * 512 + hf * 256) = v;
        }
    }
  }
  STAMP(25); RSTAMP(31);
  STAMP_FLUSH;
}

__global__ __launch_bounds__(512, 2) void attn_kernel(const uint16_t* __restrict__ x, const uint16_t* __restrict__ kc,
                                                      const uint16_t* __restrict__ vc, const uint16_t* __restrict__ wq,
                                                      uint16_t* __restrict__ ows) {
  extern __shared__ __attribute__((aligned(16))) char smem[];
  const int lane = threadIdx.x & 63;
  const int wid = __builtin_amdgcn_readfirstlane(threadIdx.x >> 6);
  const int h = blockIdx.x & 31, b = blockIdx.x >> 5;
  const unsigned lds0 = (unsigned)(size_t)smem;
  attn_body(x, kc, vc, wq, ows, lds0, wid, lane, b, h);
  if (threadIdx.x == 0) *(unsigned*)((char*)ows + ((size_t)8 << 20) + (size_t)blockIdx.x * 128) = 0u;
}

constexpr int KSLOT = 13312;
constexpr size_t WS_CNT = (size_t)8 << 20;
constexpr size_t WS_X = (size_t)9 << 20;
template <bool LOADER>
DI void oproj_body(const uint16_t* __restrict__ ows, const uint16_t* __restrict__ wo, uint16_t* __restrict__ out, char* __restrict__ ws,
                   unsigned lds0, int wid, int lane) {
  const int xcd = blockIdx.x & 7, idx = blockIdx.x >> 3;
  const int kq = xcd >> 1, mh = xcd & 1, mgi = idx / 5, nb = idx % 5;
  const int mt0 = 28 * mh + (mgi < 4 ? 5 * mgi : 20 + 4 * (mgi - 4)), mcnt = mgi < 4 ? 5 : 4;
  const int r = lane & 15, g = lane >> 4, q4 = r >> 2, p = lane & 3;
  const int rot = (mgi + nb) & 7;
  const unsigned o_voff = (unsigned)lane * 16;
  if constexpr (LOADER) {
    const int lj = wid - 4;
    const unsigned w_voff0 = (unsigned)((g * DM + 8 * (r ^ (g << 1))) * 2), w_voff1 = (unsigned)((g * DM + 8 * (r ^ ((4 | g) << 1))) * 2);
    const uint16_t* wo_s = wo + (size_t)(1024 * kq + 32 * lj) * DM + 128 * nb;
    const uint16_t* o_s = ows + ((size_t)mt0 * 128 + 32 * kq + lj) * 512;
    auto issue = [&](int u) {
      const int uu = (u + rot) & 7;
      const unsigned d = lds0 + ((u % 3) * 4 + lj) * KSLOT;
#pragma unroll
      for (int i = 0; i < 8; ++i) {
        dma1(d + i * 1024, (i & 1) ? w_voff1 : w_voff0, wo_s + (size_t)(128 * uu + 4 * i) * DM);
        if (i < 5) dma1(d + 8192 + i * 1024, o_voff, o_s + ((size_t)(i < mcnt ? i : mcnt - 1) * 128 + 4 * uu) * 512);
      }
    };
    issue(0); issue(1);
#pragma unroll
    for (int u = 0; u < 8; ++u) {
      if (u < 7) waitv<13>(); else waitv<0>();
      BARRIER();
      if (u + 2 < 8) issue(u + 2);
    }
    return;
  } else {
  const int np = wid;
  const int trow = 16 * (g >> 1) + 4 * (g & 1) + q4;
  const unsigned tra = lds0 + (unsigned)(trow * 256 + (((2 * np) ^ (trow & 7)) << 5) + 8 * p);
  const unsigned ofa = lds0 + 8192 + lane * 16;
  f32x4 acc[2][5];
#pragma unroll
  for (int c = 0; c < 2; ++c)
#pragma unroll
    for (int m = 0; m < 5; ++m) acc[c][m] = f32x4{0.f, 0.f, 0.f, 0.f};
  bf16x8 af[2][2], bf[2][5];
#pragma unroll
  for (int u = 0; u < 8; ++u) {
    BARRIER();
    const unsigned ub = (u % 3) * 4 * KSLOT;
    unsigned t0 = tra + ub, t1 = (tra ^ 32) + ub, ob = ofa + ub;
    OPAQUE(t0); OPAQUE(t1); OPAQUE(ob);
    SCHED();
#pragma unroll
    for (int j = 0; j < 4; ++j) {
#pragma unroll
      for (int m = 0; m < 5; ++m) {
        if (u > 0 || j > 0) {
          acc[0][m] = mfma16(af[(j + 1) & 1][0], bf[(j + 1) & 1][m], acc[0][m]);
          acc[1][m] = mfma16(af[(j + 1) & 1][1], bf[(j + 1) & 1][m], acc[1][m]);
        }
        SCHED();
        if (m == 0) af[j & 1][0] = trpair(t0 + j * KSLOT, t0 + j * KSLOT + 2048);
        if (m == 1) af[j & 1][1] = trpair(t1 + j * KSLOT, t1 + j * KSLOT + 2048);
        if (m < 2) bf[j & 1][m] = ldsread16(ob + j * KSLOT + m * 1024);
        if (m == 2) { bf[j & 1][2] = ldsread16(ob + j * KSLOT + 2048); bf[j & 1][3] = ldsread16(ob + j * KSLOT + 3072); bf[j & 1][4] = ldsread16(ob + j * KSLOT + 4096); }
        SCHED();
      }
    }
  }
#pragma unroll
  for (int m = 0; m < 5; ++m) {
    acc[0][m] = mfma16(af[1][0], bf[1][m], acc[0][m]);
    acc[1][m] = mfma16(af[1][1], bf[1][m], acc[1][m]);
  }
  const int dest = (idx << 3) | (np << 1) | mh;
  if (np != kq) {
    char* xp = ws + WS_X + ((size_t)(dest * 4 + kq) * 5) * 1024;
#pragma unroll
    for (int m = 0; m < 5; ++m) {
      u32x4 v = {cvtpk(acc[0][m][0], acc[0][m][1]), cvtpk(acc[0][m][2], acc[0][m][3]), cvtpk(acc[1][m][0], acc[1][m][1]), cvtpk(acc[1][m][2], acc[1][m][3])};
      asm volatile("global_store_dwordx4 %0, %1, %2 offset:0 sc1\n\ts_nop 1" :: "v"(o_voff + (unsigned)(m * 1024)), "v"(v), "s"(xp) : "memory");
    }
    asm volatile("s_waitcnt vmcnt(0)" ::: "memory");
    if (lane == 0) __hip_atomic_fetch_add((unsigned*)(ws + WS_CNT + (size_t)dest * 128), 1u, __ATOMIC_RELAXED, __HIP_MEMORY_SCOPE_AGENT);
    return;
  }
  {
    unsigned* cnt = (unsigned*)(ws + WS_CNT + (size_t)blockIdx.x * 128);
    for (int spin = 0; spin < (1 << 22); ++spin) {
      if (__hip_atomic_load(cnt, __ATOMIC_RELAXED, __HIP_MEMORY_SCOPE_AGENT) >= 3u) break;
      __builtin_amdgcn_s_sleep(1);
    }
    asm volatile("s_barrier" ::: "memory");
    const char* xp = ws + WS_X + ((size_t)(blockIdx.x * 4) * 5) * 1024;
    u32x4 v[3][5];
#pragma unroll
    for (int s = 0; s < 3; ++s)
#pragma unroll
      for (int m = 0; m < 5; ++m)
        asm volatile("global_load_dwordx4 %0, %1, %2 offset:0 sc1" : "=v"(v[s][m]) : "v"(o_voff + (unsigned)(m * 1024)), "s"(xp + (size_t)((kq + s + 1) & 3) * 5120) : "memory");
    asm volatile("s_waitcnt vmcnt(0)" : "+v"(v[0][0]), "+v"(v[0][1]), "+v"(v[0][2]), "+v"(v[0][3]), "+v"(v[0][4]),
                 "+v"(v[1][0]), "+v"(v[1][1]), "+v"(v[1][2]), "+v"(v[1][3]), "+v"(v[1][4]),
                 "+v"(v[2][0]), "+v"(v[2][1]), "+v"(v[2][2]), "+v"(v[2][3]), "+v"(v[2][4]) :: "memory");
#pragma unroll
    for (int m = 0; m < 5; ++m)
#pragma unroll
      for (int c = 0; c < 2; ++c) {
        f32x4 t = acc[c][m];
#pragma unroll
        for (int s = 0; s < 3; ++s) {
          const unsigned w0 = v[s][m][2 * c], w1 = v[s][m][2 * c + 1];
          t += f32x4{__uint_as_float(w0 << 16), __uint_as_float(w0 & 0xffff0000u), __uint_as_float(w1 << 16), __uint_as_float(w1 & 0xffff0000u)};
        }
        if (m < mcnt) {
          u32x2 o = {cvtpk(t[0], t[1]), cvtpk(t[2], t[3])};
          *(u32x2*)(out + (size_t)((mt0 + m) * 16 + r) * DM + 128 * nb + 16 * (2 * np + c) + 4 * g) = o;
        }
      }
  }
  }
}
__global__ __launch_bounds__(512, 2) void oproj_kernel(const uint16_t* __restrict__ ows, const uint16_t* __restrict__ wo,
                                                       uint16_t* __restrict__ out, char* __restrict__ ws) {
  extern __shared__ __attribute__((aligned(16))) char smem[];
  const int lane = threadIdx.x & 63;
  const int wid = __builtin_amdgcn_readfirstlane(threadIdx.x >> 6);
  const unsigned lds0 = (unsigned)(size_t)smem;
  if (wid < 4) oproj_body<false>(ows, wo, out, ws, lds0, wid, lane);
  else oproj_body<true>(ows, wo, out, ws, lds0, wid, lane);
}

extern "C" void kernel_launch(void* const* d_in, const int* in_sizes, int n_in, void* d_out, int out_size, void* d_ws,
                              size_t ws_size, hipStream_t stream) {
  const uint16_t* x = (const uint16_t*)d_in[0];
  const uint16_t* kc = (const uint16_t*)d_in[1];
  const uint16_t* vc = (const uint16_t*)d_in[2];
  const uint16_t* wq = (const uint16_t*)d_in[3];
  const uint16_t* wo = (const uint16_t*)d_in[4];
  uint16_t* ows = (uint16_t*)d_ws;
  hipFuncSetAttribute((const void*)attn_kernel, hipFuncAttributeMaxDynamicSharedMemorySize, 10 * SLOT);
  hipFuncSetAttribute((const void*)oproj_kernel, hipFuncAttributeMaxDynamicSharedMemorySize, 12 * KSLOT);
  attn_kernel<<<256, 512, 10 * SLOT, stream>>>(x, kc, vc, wq, ows);
  oproj_kernel<<<240, 512, 12 * KSLOT, stream>>>(ows, wo, (uint16_t*)d_out, (char*)d_ws);
}
```

```cpp
#include <hip/hip_runtime.h>
#include <hip/hip_bf16.h>
#include <stdint.h>

typedef __attribute__((ext_vector_type(8))) short bf16x8;
typedef __attribute__((ext_vector_type(4))) short s16x4;
typedef __attribute__((ext_vector_type(4))) float f32x4;
typedef __attribute__((ext_vector_type(4))) unsigned u32x4;
typedef __attribute__((ext_vector_type(2))) unsigned u32x2;
#define LDSP(T) __attribute__((address_space(3))) T*
#define DI __device__ __forceinline__

constexpr int NB = 8, SQ = 112, DM = 640, NH = 32, SK = 512, DH = 128;
constexpr int SLOT = 16384;

typedef __attribute__((ext_vector_type(2))) float f32x2;
typedef __attribute__((ext_vector_type(2))) __bf16 bf16x2;
DI unsigned cvtpk(float lo, float hi) {
  f32x2 v = {lo, hi};
  return __builtin_bit_cast(unsigned, __builtin_convertvector(v, bf16x2));
}
DI f32x4 mfma16(bf16x8 a, bf16x8 b, f32x4 c) { return __builtin_amdgcn_mfma_f32_16x16x32_bf16(a, b, c, 0, 0, 0); }
DI s16x4 trread(unsigned addr) { return __builtin_amdgcn_ds_read_tr16_b64_v4i16((LDSP(s16x4))(size_t)addr); }
DI bf16x8 trpair(unsigned a0, unsigned a1) {
  s16x4 lo = trread(a0), hi = trread(a1);
  return __builtin_shufflevector(lo, hi, 0, 1, 2, 3, 4, 5, 6, 7);
}
DI bf16x8 ldsread16(unsigned addr) { return *(LDSP(bf16x8))(size_t)addr; }
DI void ldswrite16(unsigned addr, bf16x8 v) { *(LDSP(bf16x8))(size_t)addr = v; }

template <int OFF> DI void gload(bf16x8& dst, unsigned voff, const void* sbase) {
  asm volatile("global_load_dwordx4 %0, %1, %2 offset:%3" : "=v"(dst) : "v"(voff), "s"(sbase), "i"(OFF) : "memory");
}
DI void dma2(unsigned m0val, unsigned voff, const void* sbase) {
  unsigned keep;
  asm volatile("s_mov_b32 %0, m0\n\ts_mov_b32 m0, %1\n\ts_nop 0\n\t"
               "global_load_lds_dwordx4 %2, %3 nt\n\tglobal_load_lds_dwordx4 %2, %3 offset:1024 nt\n\t"
               "s_mov_b32 m0, %0"
               : "=&s"(keep) : "s"(m0val), "v"(voff), "s"(sbase) : "memory");
}
DI void dma1(unsigned m0val, unsigned voff, const void* sbase) {
  unsigned keep;
  asm volatile("s_mov_b32 %0, m0\n\ts_mov_b32 m0, %1\n\ts_nop 0\n\t"
               "global_load_lds_dwordx4 %2, %3\n\t"
               "s_mov_b32 m0, %0"
               : "=&s"(keep) : "s"(m0val), "v"(voff), "s"(sbase) : "memory");
}
#define STAMP_DECL
#define STAMP(i)
#define RSTAMP(i)
#define STAMP_FLUSH
#define BARRIER() asm volatile("s_waitcnt lgkmcnt(0)\n\ts_barrier" ::: "memory")
template <int N> DI void waitv() { asm volatile("s_waitcnt vmcnt(%0)" ::"i"(N) : "memory"); }
template <int N> DI void waitv2(bf16x8& a, bf16x8& b) { asm volatile("s_waitcnt vmcnt(%2)" : "+v"(a), "+v"(b) : "i"(N) : "memory"); }
template <int N> DI void waitv4(bf16x8& a, bf16x8& b, bf16x8& c, bf16x8& d) {
  asm volatile("s_waitcnt vmcnt(%4)" : "+v"(a), "+v"(b), "+v"(c), "+v"(d) : "i"(N) : "memory");
}

template <int N> DI void waitv6(bf16x8& a, bf16x8& b, bf16x8& c, bf16x8& d, bf16x8& e, bf16x8& f) {
  asm volatile("s_waitcnt vmcnt(%6)" : "+v"(a), "+v"(b), "+v"(c), "+v"(d), "+v"(e), "+v"(f) : "i"(N) : "memory");
}
template <int N> DI void waitv3(bf16x8& a, bf16x8& b, bf16x8& c) {
  asm volatile("s_waitcnt vmcnt(%3)" : "+v"(a), "+v"(b), "+v"(c) : "i"(N) : "memory");
}
DI void dma4(unsigned m0val, unsigned voff, const void* sbase) {
  unsigned keep;
  asm volatile("s_mov_b32 %0, m0\n\ts_mov_b32 m0, %1\n\ts_nop 0\n\t"
               "global_load_lds_dwordx4 %2, %3\n\tglobal_load_lds_dwordx4 %2, %3 offset:1024\n\t"
               "global_load_lds_dwordx4 %2, %3 offset:2048\n\tglobal_load_lds_dwordx4 %2, %3 offset:3072\n\t"
               "s_mov_b32 m0, %0"
               : "=&s"(keep) : "s"(m0val), "v"(voff), "s"(sbase) : "memory");
}
#define OPAQUE(x) asm volatile("" : "+v"(x))
#define SCHED() __builtin_amdgcn_sched_barrier(0)

typedef __attribute__((ext_vector_type(16))) float f32x16;
DI f32x16 mfma32(bf16x8 a, bf16x8 b, f32x16 c) { return __builtin_amdgcn_mfma_f32_32x32x16_bf16(a, b, c, 0, 0, 0); }
template <int OFF> DI void gload_nt(bf16x8& dst, unsigned voff, const void* sbase) {
  asm volatile("global_load_dwordx4 %0, %1, %2 offset:%3 nt" : "=v"(dst) : "v"(voff), "s"(sbase), "i"(OFF) : "memory");
}
DI void dma1nt(unsigned m0val, unsigned voff, const void* sbase) {
  unsigned keep;
  asm volatile("s_mov_b32 %0, m0\n\ts_mov_b32 m0, %1\n\ts_nop 0\n\t"
               "global_load_lds_dwordx4 %2, %3 nt\n\t"
               "s_mov_b32 m0, %0"
               : "=&s"(keep) : "s"(m0val), "v"(voff), "s"(sbase) : "memory");
}
DI f32x4 ldsreadf(unsigned addr) { return *(LDSP(f32x4))(size_t)addr; }
DI void ldswritef(unsigned addr, f32x4 v) { *(LDSP(f32x4))(size_t)addr = v; }
DI f32x4 q4of(const f32x16& v, int i) { return f32x4{v[4 * i], v[4 * i + 1], v[4 * i + 2], v[4 * i + 3]}; }

__host__ __device__ constexpr int vslot(int t) { return t < 2 ? 8 + t : t - 2; }

DI void attn_body(const uint16_t* __restrict__ x, const uint16_t* __restrict__ kc, const uint16_t* __restrict__ vc,
                  const uint16_t* __restrict__ wq, uint16_t* __restrict__ ows, unsigned lds0, int wid, int lane, int b, int h) {
  const int r = lane & 15, g = lane >> 4;
  const int q = lane & 31, hh = lane >> 5;
  const int q4 = r >> 2, p = lane & 3, blk = g & 1;
  const int qg = wid & 3, kh = wid >> 2;
  STAMP_DECL RSTAMP(30); STAMP(0);
  const int rot = (b + 3 * (h >> 3)) % 10;
  const uint16_t* wq_s = wq + h * DH + (size_t)(8 * wid) * 4096;
  const uint16_t* x_s = x + (size_t)(b * SQ + 16 * (wid < 7 ? wid : 6)) * DM;
  const uint16_t* kb = kc + (size_t)(b * NH + h) * SK * DH + 8 * wid * DH;
  const uint16_t* vb = vc + (size_t)(b * NH + h) * SK * DH + 8 * wid * DH;
  auto unitof = [&](int hs) { int u = hs + rot; return u >= 10 ? u - 10 : u; };
  const unsigned w_voff = (unsigned)((g * 4096 + 8 * (r ^ (g << 2))) * 2);
  const unsigned v_voff = (unsigned)((g * DH + 8 * (r ^ (g << 2))) * 2);
  const unsigned k_voff0 = (unsigned)((g * DH + 8 * (r ^ (8 * (wid & 1) + g))) * 2);
  const unsigned k_voff1 = (unsigned)(((4 + g) * DH + 8 * (r ^ (8 * (wid & 1) + 4 + g))) * 2);
  const unsigned x_voff0 = (unsigned)(((lane >> 3) * DM + 8 * ((lane & 7) ^ (lane >> 4))) * 2);
  const unsigned x_voff1 = (unsigned)(((8 + (lane >> 3)) * DM + 8 * ((lane & 7) ^ (4 | (lane >> 4)))) * 2);
  auto dmaU1 = [&](int hs, int j) {
    const unsigned d = lds0 + 2 * (hs % 5) * SLOT + wid * 2048 + (j >> 1) * 1024;
    const int u = unitof(hs);
    if ((j & 1) == 0) dma1nt(d, w_voff, wq_s + (size_t)(64 * u + 4 * (j >> 1)) * 4096);
    else dma1(d + SLOT, (j >> 1) ? x_voff1 : x_voff0, x_s + 64 * u);
  };
  auto dmaU = [&](int hs) { dmaU1(hs, 0); dmaU1(hs, 1); dmaU1(hs, 2); dmaU1(hs, 3); };
  auto dmaK1 = [&](int t, int n) { dma1nt(lds0 + t * SLOT + wid * 2048 + n * 1024, n ? k_voff1 : k_voff0, kb + t * 64 * DH); };
  auto dmaV1 = [&](int t, int n) { dma1nt(lds0 + vslot(t) * SLOT + wid * 2048 + n * 1024, v_voff, vb + (t * 64 + 4 * n) * DH); };
  dmaU(0);
  asm volatile("s_barrier" ::: "memory");
  dmaU(1);
  STAMP(1);
  const unsigned tr0 = lds0 + (unsigned)((8 * hh + q4) * 256 + (q4 << 6) + 32 * blk + 8 * p);
  unsigned trb[4];
  const int xrow = 32 * qg + q;
  const unsigned xb = lds0 + SLOT + (unsigned)(xrow * 128 + ((hh ^ ((xrow >> 1) & 7)) << 4));

  f32x16 qa[2];
#pragma unroll
  for (int c = 0; c < 2; ++c)
#pragma unroll
    for (int i = 0; i < 16; ++i) qa[c][i] = 0.f;
  bf16x8 af[2][8], xf[2][4];
  auto dmaT = [&](int i) {
    if (i < 8) { dmaK1(i, 0); dmaK1(i, 1); } else if (i < 16) { dmaV1(i - 8, 0); dmaV1(i - 8, 1); }
  };
#pragma unroll
  for (int hs = 0; hs < 10; ++hs) {
    if (hs <= 8) waitv<4>(); else waitv<2>();
    if (hs == 0) STAMP(2);
    BARRIER();
    if (hs == 2) STAMP(3); if (hs == 6) STAMP(4);
#pragma unroll
    for (int c = 0; c < 2; ++c) { trb[c] = (tr0 ^ ((2 * kh + c) << 6)) + 2 * (hs % 5) * SLOT; OPAQUE(trb[c]); }
    const unsigned xa = xb + 2 * (hs % 5) * SLOT;
    SCHED();
#pragma unroll
    for (int d = 0; d < 8; ++d) {
      if (hs > 0) qa[d & 1] = mfma32(af[(hs - 1) & 1][d], xf[(hs - 1) & 1][d >> 1], qa[d & 1]);
      SCHED();
      if (d < 4) xf[hs & 1][d] = ldsread16(xa ^ (32 * d));
      if (d < 4) af[hs & 1][d] = trpair(trb[d & 1] + 4096 * (d >> 1), trb[d & 1] + 4096 * (d >> 1) + 1024);
      else if (d < 6) {
        af[hs & 1][2 * d - 4] = trpair(trb[0] + 4096 * (d - 2), trb[0] + 4096 * (d - 2) + 1024);
        af[hs & 1][2 * d - 3] = trpair(trb[1] + 4096 * (d - 2), trb[1] + 4096 * (d - 2) + 1024);
      }
      if ((d & 1) == 1 && hs + 2 < 10) dmaU1(hs + 2, d >> 1);
      if (d < 2 && hs >= 8) dmaK1(hs - 8, d);
      SCHED();
    }
  }
#pragma unroll
  for (int d = 0; d < 8; ++d) qa[d & 1] = mfma32(af[1][d], xf[1][d >> 1], qa[d & 1]);
  dmaT(2);
  constexpr float C = 0.08838834764831845f * 1.4426950408889634f;
  qa[0] *= C; qa[1] *= C;
  bf16x8 qf[8];
  {
    const unsigned mine = lds0 + 6 * SLOT + wid * 4096 + lane * 16, other = lds0 + 6 * SLOT + (wid ^ 4) * 4096 + lane * 16;
    bf16x8 qm[4];
#pragma unroll
    for (int c = 0; c < 2; ++c)
#pragma unroll
      for (int s = 0; s < 2; ++s) {
        unsigned a0 = cvtpk(qa[c][8 * s], qa[c][8 * s + 1]), a1 = cvtpk(qa[c][8 * s + 2], qa[c][8 * s + 3]);
        unsigned b0 = cvtpk(qa[c][8 * s + 4], qa[c][8 * s + 5]), b1 = cvtpk(qa[c][8 * s + 6], qa[c][8 * s + 7]);
        auto s0 = __builtin_amdgcn_permlane32_swap(a0, b0, false, false);
        auto s1 = __builtin_amdgcn_permlane32_swap(a1, b1, false, false);
        u32x4 v = {s0[0], s1[0], s0[1], s1[1]};
        qm[2 * c + s] = __builtin_bit_cast(bf16x8, v);
        ldswrite16(mine + (2 * c + s) * 1024, qm[2 * c + s]);
      }
    BARRIER();
    dmaT(3);
#pragma unroll
    for (int i = 0; i < 4; ++i) {
      const bf16x8 o = ldsread16(other + i * 1024);
      qf[i] = kh ? o : qm[i];
      qf[4 + i] = kh ? qm[i] : o;
    }
  }
  STAMP(5);
  constexpr float THR = 8.0f;
  const int key = (q & 19) | ((q & 4) << 1) | ((q & 8) >> 1);
  const unsigned kr0 = lds0 + kh * 8192 + (unsigned)(key * 256 + ((hh ^ (key & 15)) << 4));
  unsigned krb[8];
  bf16x8 kf[2][8];
  f32x16 sc[2], scn, minit;
#pragma unroll
  for (int i = 0; i < 16; ++i) minit[i] = 0.f;
  bf16x8 pf[8][2];
  float alpha[8];
  unsigned resc = 0;
  float mref = 0.f, off = 0.f;
  bool offf = false;
  auto sm_max = [&](int t) {
    f32x16& v = sc[t & 1];
    float m0 = fmaxf(fmaxf(v[0], v[1]), v[2]), m1 = fmaxf(fmaxf(v[3], v[4]), v[5]);
    m0 = fmaxf(fmaxf(m0, v[6]), v[7]); m1 = fmaxf(fmaxf(m1, v[8]), v[9]);
    m0 = fmaxf(fmaxf(m0, v[10]), v[11]); m1 = fmaxf(fmaxf(m1, v[12]), v[13]);
    m0 = fmaxf(fmaxf(m0, v[14]), v[15]);
    m0 = fmaxf(m0, m1);
    auto sw = __builtin_amdgcn_permlane32_swap(__float_as_uint(m0), __float_as_uint(m0), false, false);
    m0 = fmaxf(__uint_as_float(sw[0]), __uint_as_float(sw[1]));
    alpha[t] = 1.f;
    m0 -= off;
    if (t <= 1 || __builtin_expect(offf || __any(m0 > THR), 0)) {
      const bool upd = t == 0 || __any(m0 > THR);
      const float dl = t == 0 ? m0 : (upd ? fmaxf(m0, 0.f) : 0.f);
      if (t > 0 && upd) { alpha[t] = __builtin_amdgcn_exp2f(-dl); resc |= 1u << t; }
      mref += dl;
      v -= off + dl;
      off = dl; offf = upd;
#pragma unroll
      for (int i = 0; i < 16; ++i) minit[i] = -mref;
    }
  };
  unsigned pw[8][8];
  float sh[8][8];
  auto sm_exp = [&](int t, int j) {
    float e[4];
#pragma unroll
    for (int i = 0; i < 4; ++i) e[i] = __builtin_amdgcn_exp2f(sc[t & 1][4 * j + i]);
    pw[t][2 * j] = cvtpk(e[0], e[1]);
    pw[t][2 * j + 1] = cvtpk(e[2], e[3]);
    asm volatile("" : "+v"(pw[t][2 * j]), "+v"(pw[t][2 * j + 1]));
  };
#pragma unroll
  for (int t = 0; t < 10; ++t) {
    if (t < 8) {
      waitv<6>();
      BARRIER();
      STAMP(6 + t);
      if ((t & 3) == 0) {
#pragma unroll
        for (int s = 0; s < 8; ++s) { krb[s] = (kr0 ^ (s << 5)) + t * SLOT; OPAQUE(krb[s]); }
      }
    }
    SCHED();
#pragma unroll
    for (int s = 0; s < 8; ++s) {
      if (t >= 1 && t <= 8) {
        if (s == 0) scn = mfma32(kf[(t - 1) & 1][0], qf[0], minit);
        else scn = mfma32(kf[(t - 1) & 1][s], qf[s], scn);
      }
      SCHED();
      if (t < 8 && s < 4) {
        kf[t & 1][2 * s] = ldsread16(krb[2 * s] + (t & 3) * SLOT);
        kf[t & 1][2 * s + 1] = ldsread16(krb[2 * s + 1] + (t & 3) * SLOT);
      }
      if (t >= 2 && s == 1) sm_max(t - 2);
      if (t >= 2 && (s == 3 || s == 5)) sm_exp(t - 2, (s - 3) >> 1);
      if (t >= 2 && s == 2) {
#pragma unroll
        for (int i = 0; i < 8; ++i) sh[t - 2][i] = sc[t & 1][8 + i];
      }
      SCHED();
    }
    if (t >= 1 && t <= 8) sc[(t - 1) & 1] = scn;
    if (t < 8) dmaT(4 + t);
  }
#pragma unroll
  for (int t = 0; t < 8; ++t) {
    u32x4 v = {pw[t][0], pw[t][1], pw[t][2], pw[t][3]};
    pf[t][0] = __builtin_bit_cast(bf16x8, v);
  }
  auto sm_exp2 = [&](int t, int j) {
    float e[4];
#pragma unroll
    for (int i = 0; i < 4; ++i) e[i] = __builtin_amdgcn_exp2f(sh[t][4 * j + i]);
    pw[t][4 + 2 * j] = cvtpk(e[0], e[1]);
    pw[t][5 + 2 * j] = cvtpk(e[2], e[3]);
    asm volatile("" : "+v"(pw[t][4 + 2 * j]), "+v"(pw[t][5 + 2 * j]));
    if (j == 1) {
      u32x4 v = {pw[t][4], pw[t][5], pw[t][6], pw[t][7]};
      pf[t][1] = __builtin_bit_cast(bf16x8, v);
    }
  };
  STAMP(14);
  f32x16 oa[4];
#pragma unroll
  for (int c = 0; c < 4; ++c)
#pragma unroll
    for (int i = 0; i < 16; ++i) oa[c][i] = 0.f;
  f32x16 la;
#pragma unroll
  for (int i = 0; i < 16; ++i) la[i] = 0.f;
  const bf16x8 ones = {0x3F80, 0x3F80, 0x3F80, 0x3F80, 0x3F80, 0x3F80, 0x3F80, 0x3F80};
  const int cs = 2 * (kh ^ 1);
#pragma unroll
  for (int t = 0; t < 8; ++t) {
    switch (t) {
      case 0: case 1: case 2: case 3: case 4: waitv<6>(); break;
      case 5: waitv<4>(); break;
      case 6: waitv<2>(); break;
      case 7: waitv<0>(); break;
    }
    BARRIER();
    STAMP(16 + t);
    if (t == 0 || t == 2 || t == 6) {
#pragma unroll
      for (int c = 0; c < 4; ++c) { trb[c] = (tr0 ^ ((c ^ cs) << 6)) + kh * 8192 + (t == 0 ? 8 * SLOT : t == 2 ? 0 : 4 * SLOT); OPAQUE(trb[c]); }
    }
    const unsigned so = (t < 2 ? t : t < 6 ? t - 2 : t - 6) * SLOT;
    if (t > 1) {
      if (__builtin_expect((resc >> (t - 1)) & 1, 0)) {
#pragma unroll
        for (int c = 0; c < 4; ++c) oa[c] *= alpha[t - 1];
        la *= alpha[t - 1];
      }
    }
    SCHED();
#pragma unroll
    for (int i = 0; i < 8; ++i) {
      if (t > 0) oa[i >> 1] = mfma32(af[(t - 1) & 1][i], pf[t - 1][i & 1], oa[i >> 1]);
      SCHED();
      if (i < 4) af[t & 1][i] = trpair(trb[i >> 1] + so + 4096 * (i & 1), trb[i >> 1] + so + 4096 * (i & 1) + 1024);
      else if (i < 6) {
        af[t & 1][2 * i - 4] = trpair(trb[i - 2] + so, trb[i - 2] + so + 1024);
        af[t & 1][2 * i - 3] = trpair(trb[i - 2] + so + 4096, trb[i - 2] + so + 4096 + 1024);
      }
      if (i == 2 || i == 6) sm_exp2(t, i >> 2);
      SCHED();
    }
    if (t > 0) { la = mfma32(ones, pf[t - 1][0], la); la = mfma32(ones, pf[t - 1][1], la); }
    SCHED();
    dmaT(12 + t);
  }
  if (__builtin_expect((resc >> 7) & 1, 0)) {
#pragma unroll
    for (int c = 0; c < 4; ++c) oa[c] *= alpha[7];
    la *= alpha[7];
  }
  la = mfma32(ones, pf[7][0], la); la = mfma32(ones, pf[7][1], la);
#pragma unroll
  for (int i = 0; i < 4; ++i) oa[i >> 1] = mfma32(af[1][i], pf[7][i & 1], oa[i >> 1]);
  STAMP(24);
  {
    const float l = la[0];
    const unsigned mine = lds0 + 6 * SLOT + wid * 4096 + lane * 16, other = lds0 + 6 * SLOT + (wid ^ 4) * 4096 + lane * 16;
    const unsigned mlm = lds0 + wid * 256 + q * 8, mlo = lds0 + (wid ^ 4) * 256 + q * 8;
    *(LDSP(f32x2))(size_t)mlm = f32x2{mref, l};
#pragma unroll
    for (int i = 0; i < 4; ++i) oa[2 + (i >> 1)] = mfma32(af[1][4 + i], pf[7][i & 1], oa[2 + (i >> 1)]);
#pragma unroll
    for (int c = 0; c < 2; ++c)
#pragma unroll
      for (int hf = 0; hf < 2; ++hf) {
        u32x4 v = {cvtpk(oa[c][8 * hf], oa[c][8 * hf + 1]), cvtpk(oa[c][8 * hf + 2], oa[c][8 * hf + 3]),
                   cvtpk(oa[c][8 * hf + 4], oa[c][8 * hf + 5]), cvtpk(oa[c][8 * hf + 6], oa[c][8 * hf + 7])};
        ldswrite16(mine + (2 * c + hf) * 1024, __builtin_bit_cast(bf16x8, v));
      }
    BARRIER();
    const f32x2 ml = *(LDSP(f32x2))(size_t)mlo;
    u32x4 pv[4];
#pragma unroll
    for (int i = 0; i < 4; ++i) pv[i] = __builtin_bit_cast(u32x4, ldsread16(other + i * 1024));
    const float m = fmaxf(mref, ml[0]);
    const float f0 = __builtin_amdgcn_exp2f(mref - m), f1 = __builtin_amdgcn_exp2f(ml[0] - m);
    const float linv = __builtin_amdgcn_rcpf(l * f0 + ml[1] * f1);
    const float s0 = f0 * linv, s1 = f1 * linv;
    uint16_t* op = ows + ((size_t)((b * 7 + 2 * qg + (q >> 4)) * 128 + h * 4 + 2 * kh) * 512) + (hh * 16 + (q & 15)) * 8;
    if (xrow < SQ) {
#pragma unroll
      for (int c = 0; c < 2; ++c)
#pragma unroll
        for (int hf = 0; hf < 2; ++hf) {
          u32x4 v;
#pragma unroll
          for (int i = 0; i < 4; ++i) {
            const unsigned w = pv[2 * c + hf][i];
            const float lo = fmaf(oa[2 + c][8 * hf + 2 * i], s0, __uint_as_float(w << 16) * s1);
            const float hi = fmaf(oa[2 + c][8 * hf + 2 * i + 1], s0, __uint_as_float(w & 0xffff0000u) * s1);
            v[i] = cvtpk(lo, hi);
          }
          asm volatile("global_store_dwordx4 %0, %1, off sc1\n\ts_nop 1" :: "v"(op + c * 512 + hf * 256), "v"(v) : "memory");
        }
    }
  }
  STAMP(25); RSTAMP(31);
  STAMP_FLUSH;
}

extern "C" __device__ unsigned long long amd_dispatch_id() __asm("llvm.amdgcn.dispatch.id");
typedef unsigned long long u64;
constexpr int KSLOT = 13312;
constexpr size_t WS_FO = (size_t)8 << 20;
constexpr size_t WS_FX = WS_FO + 65536;
constexpr size_t WS_X = (size_t)9 << 20;
template <bool LOADER>
DI void oproj_body(const uint16_t* __restrict__ ows, const uint16_t* __restrict__ wo, uint16_t* __restrict__ out, char* __restrict__ ws,
                   unsigned lds0, int wid, int lane, u64 tag) {
  const int xcd = blockIdx.x & 7, idx = blockIdx.x >> 3;
  const int kq = xcd >> 1, mh = xcd & 1, mgi = idx / 5, nb = idx % 5;
  const int mt0 = 28 * mh + (mgi < 4 ? 5 * mgi : 20 + 4 * (mgi - 4)), mcnt = mgi < 4 ? 5 : 4;
  const int r = lane & 15, g = lane >> 4, q4 = r >> 2, p = lane & 3;
  const int rot = (mgi + nb) & 7;
  const unsigned o_voff = (unsigned)lane * 16;
  if constexpr (LOADER) {
    const int lj = wid - 4;
    const unsigned w_voff0 = (unsigned)((g * DM + 8 * (r ^ (g << 1))) * 2), w_voff1 = (unsigned)((g * DM + 8 * (r ^ ((4 | g) << 1))) * 2);
    const uint16_t* wo_s = wo + (size_t)(1024 * kq + 32 * lj) * DM + 128 * nb;
    const uint16_t* o_s = ows + ((size_t)mt0 * 128 + 32 * kq + lj) * 512;
    auto issue = [&](int u, bool dw, bool dz) {
      const int uu = (u + rot) & 7;
      const unsigned d = lds0 + ((u % 3) * 4 + lj) * KSLOT;
#pragma unroll
      for (int i = 0; i < 8; ++i) {
        if (dw) dma1(d + i * 1024, (i & 1) ? w_voff1 : w_voff0, wo_s + (size_t)(128 * uu + 4 * i) * DM);
        if (dz && i < 5) dma1(d + 8192 + i * 1024, o_voff, o_s + ((size_t)(i < mcnt ? i : mcnt - 1) * 128 + 4 * uu) * 512);
      }
    };
    issue(0, true, false); issue(1, true, false);
    if (lj == 0) {
      const int bb = (lane & 8) ? (mt0 + mcnt - 1) / 7 : mt0 / 7;
      u64* f = (u64*)(ws + WS_FO) + bb * 32 + 8 * kq + (lane & 7);
      for (int spin = 0; spin < (1 << 22); ++spin) {
        if (__all(__hip_atomic_load(f, __ATOMIC_RELAXED, __HIP_MEMORY_SCOPE_AGENT) == tag)) break;
        __builtin_amdgcn_s_sleep(1);
      }
      __builtin_amdgcn_fence(__ATOMIC_ACQUIRE, "agent");
      asm volatile("s_waitcnt vmcnt(0)" ::: "memory");
    }
    BARRIER();
    issue(0, false, true); issue(1, false, true);
#pragma unroll
    for (int u = 0; u < 8; ++u) {
      if (u == 0) waitv<5>(); else if (u < 7) waitv<13>(); else waitv<0>();
      BARRIER();
      if (u + 2 < 8) issue(u + 2, true, true);
    }
    return;
  } else {
  const int np = wid;
  const int trow = 16 * (g >> 1) + 4 * (g & 1) + q4;
  const unsigned tra = lds0 + (unsigned)(trow * 256 + (((2 * np) ^ (trow & 7)) << 5) + 8 * p);
  const unsigned ofa = lds0 + 8192 + lane * 16;
  f32x4 acc[2][5];
#pragma unroll
  for (int c = 0; c < 2; ++c)
#pragma unroll
    for (int m = 0; m < 5; ++m) acc[c][m] = f32x4{0.f, 0.f, 0.f, 0.f};
  bf16x8 af[2][2], bf[2][5];
  BARRIER();
#pragma unroll
  for (int u = 0; u < 8; ++u) {
    BARRIER();
    const unsigned ub = (u % 3) * 4 * KSLOT;
    unsigned t0 = tra + ub, t1 = (tra ^ 32) + ub, ob = ofa + ub;
    OPAQUE(t0); OPAQUE(t1); OPAQUE(ob);
    SCHED();
#pragma unroll
    for (int j = 0; j < 4; ++j) {
#pragma unroll
      for (int m = 0; m < 5; ++m) {
        if (u > 0 || j > 0) {
          acc[0][m] = mfma16(af[(j + 1) & 1][0], bf[(j + 1) & 1][m], acc[0][m]);
          acc[1][m] = mfma16(af[(j + 1) & 1][1], bf[(j + 1) & 1][m], acc[1][m]);
        }
        SCHED();
        if (m == 0) af[j & 1][0] = trpair(t0 + j * KSLOT, t0 + j * KSLOT + 2048);
        if (m == 1) af[j & 1][1] = trpair(t1 + j * KSLOT, t1 + j * KSLOT + 2048);
        if (m < 2) bf[j & 1][m] = ldsread16(ob + j * KSLOT + m * 1024);
        if (m == 2) { bf[j & 1][2] = ldsread16(ob + j * KSLOT + 2048); bf[j & 1][3] = ldsread16(ob + j * KSLOT + 3072); bf[j & 1][4] = ldsread16(ob + j * KSLOT + 4096); }
        SCHED();
      }
    }
  }
#pragma unroll
  for (int m = 0; m < 5; ++m) {
    acc[0][m] = mfma16(af[1][0], bf[1][m], acc[0][m]);
    acc[1][m] = mfma16(af[1][1], bf[1][m], acc[1][m]);
  }
  const int dest = (idx << 3) | (np << 1) | mh;
  if (np != kq) {
    char* xp = ws + WS_X + ((size_t)(dest * 4 + kq) * 5) * 1024;
#pragma unroll
    for (int m = 0; m < 5; ++m) {
      u32x4 v = {cvtpk(acc[0][m][0], acc[0][m][1]), cvtpk(acc[0][m][2], acc[0][m][3]), cvtpk(acc[1][m][0], acc[1][m][1]), cvtpk(acc[1][m][2], acc[1][m][3])};
      asm volatile("global_store_dwordx4 %0, %1, %2 offset:0 sc1\n\ts_nop 1" :: "v"(o_voff + (unsigned)(m * 1024)), "v"(v), "s"(xp) : "memory");
    }
    asm volatile("s_waitcnt vmcnt(0)" ::: "memory");
    if (lane == 0) __hip_atomic_store((u64*)(ws + WS_FX + (size_t)dest * 128) + kq, tag, __ATOMIC_RELAXED, __HIP_MEMORY_SCOPE_AGENT);
    return;
  }
  {
    u64* f = (u64*)(ws + WS_FX + (size_t)blockIdx.x * 128) + ((kq + 1 + (lane % 3)) & 3);
    for (int spin = 0; spin < (1 << 22); ++spin) {
      if (__all(__hip_atomic_load(f, __ATOMIC_RELAXED, __HIP_MEMORY_SCOPE_AGENT) == tag)) break;
      __builtin_amdgcn_s_sleep(1);
    }
    asm volatile("s_barrier" ::: "memory");
    const char* xp = ws + WS_X + ((size_t)(blockIdx.x * 4) * 5) * 1024;
    u32x4 v[3][5];
#pragma unroll
    for (int s = 0; s < 3; ++s)
#pragma unroll
      for (int m = 0; m < 5; ++m)
        asm volatile("global_load_dwordx4 %0, %1, %2 offset:0 sc1" : "=v"(v[s][m]) : "v"(o_voff + (unsigned)(m * 1024)), "s"(xp + (size_t)((kq + s + 1) & 3) * 5120) : "memory");
    asm volatile("s_waitcnt vmcnt(0)" : "+v"(v[0][0]), "+v"(v[0][1]), "+v"(v[0][2]), "+v"(v[0][3]), "+v"(v[0][4]),
                 "+v"(v[1][0]), "+v"(v[1][1]), "+v"(v[1][2]), "+v"(v[1][3]), "+v"(v[1][4]),
                 "+v"(v[2][0]), "+v"(v[2][1]), "+v"(v[2][2]), "+v"(v[2][3]), "+v"(v[2][4]) :: "memory");
#pragma unroll
    for (int m = 0; m < 5; ++m)
#pragma unroll
      for (int c = 0; c < 2; ++c) {
        f32x4 t = acc[c][m];
#pragma unroll
        for (int s = 0; s < 3; ++s) {
          const unsigned w0 = v[s][m][2 * c], w1 = v[s][m][2 * c + 1];
          t += f32x4{__uint_as_float(w0 << 16), __uint_as_float(w0 & 0xffff0000u), __uint_as_float(w1 << 16), __uint_as_float(w1 & 0xffff0000u)};
        }
        if (m < mcnt) {
          u32x2 o = {cvtpk(t[0], t[1]), cvtpk(t[2], t[3])};
          *(u32x2*)(out + (size_t)((mt0 + m) * 16 + r) * DM + 128 * nb + 16 * (2 * np + c) + 4 * g) = o;
        }
      }
  }
  }
}
__global__ __launch_bounds__(512, 2) void fused_kernel(const uint16_t* __restrict__ x, const uint16_t* __restrict__ kc,
                                                       const uint16_t* __restrict__ vc, const uint16_t* __restrict__ wq,
                                                       const uint16_t* __restrict__ wo, uint16_t* __restrict__ out, char* __restrict__ ws) {
  extern __shared__ __attribute__((aligned(16))) char smem[];
  const int lane = threadIdx.x & 63;
  const int wid = __builtin_amdgcn_readfirstlane(threadIdx.x >> 6);
  const int h = blockIdx.x & 31, b = blockIdx.x >> 5;
  const unsigned lds0 = (unsigned)(size_t)smem;
  const u64 tag = (amd_dispatch_id() + 1) * 0x9E3779B97F4A7C15ull + (u64)(uintptr_t)__builtin_amdgcn_dispatch_ptr() * 0xC2B2AE3D27D4EB4Full + 0x165667B19E3779F9ull;
  attn_body(x, kc, vc, wq, (uint16_t*)ws, lds0, wid, lane, b, h);
  asm volatile("s_waitcnt vmcnt(0)" ::: "memory");
  BARRIER();
  if (threadIdx.x == 0) __hip_atomic_store((u64*)(ws + WS_FO) + blockIdx.x, tag, __ATOMIC_RELAXED, __HIP_MEMORY_SCOPE_AGENT);
  if (blockIdx.x >= 240) return;
  if (wid < 4) oproj_body<false>((const uint16_t*)ws, wo, out, ws, lds0, wid, lane, tag);
  else oproj_body<true>((const uint16_t*)ws, wo, out, ws, lds0, wid, lane, tag);
}

extern "C" void kernel_launch(void* const* d_in, const int* in_sizes, int n_in, void* d_out, int out_size, void* d_ws,
                              size_t ws_size, hipStream_t stream) {
  const uint16_t* x = (const uint16_t*)d_in[0];
  const uint16_t* kc = (const uint16_t*)d_in[1];
  const uint16_t* vc = (const uint16_t*)d_in[2];
  const uint16_t* wq = (const uint16_t*)d_in[3];
  const uint16_t* wo = (const uint16_t*)d_in[4];
  (void)hipFuncSetAttribute((const void*)fused_kernel, hipFuncAttributeMaxDynamicSharedMemorySize, 10 * SLOT);
  fused_kernel<<<256, 512, 10 * SLOT, stream>>>(x, kc, vc, wq, wo, (uint16_t*)d_out, (char*)d_ws);
}
```

```cpp
#include <hip/hip_runtime.h>
#include <hip/hip_bf16.h>
#include <stdint.h>

typedef __attribute__((ext_vector_type(8))) short bf16x8;
typedef __attribute__((ext_vector_type(4))) short s16x4;
typedef __attribute__((ext_vector_type(4))) float f32x4;
typedef __attribute__((ext_vector_type(4))) unsigned u32x4;
typedef __attribute__((ext_vector_type(2))) unsigned u32x2;
#define LDSP(T) __attribute__((address_space(3))) T*
#define DI __device__ __forceinline__

constexpr int NB = 8, SQ = 112, DM = 640, NH = 32, SK = 512, DH = 128;
constexpr int SLOT = 16384;

typedef __attribute__((ext_vector_type(2))) float f32x2;
typedef __attribute__((ext_vector_type(2))) __bf16 bf16x2;
DI unsigned cvtpk(float lo, float hi) {
  f32x2 v = {lo, hi};
  return __builtin_bit_cast(unsigned, __builtin_convertvector(v, bf16x2));
}
DI f32x4 mfma16(bf16x8 a, bf16x8 b, f32x4 c) { return __builtin_amdgcn_mfma_f32_16x16x32_bf16(a, b, c, 0, 0, 0); }
DI s16x4 trread(unsigned addr) { return __builtin_amdgcn_ds_read_tr16_b64_v4i16((LDSP(s16x4))(size_t)addr); }
DI bf16x8 trpair(unsigned a0, unsigned a1) {
  s16x4 lo = trread(a0), hi = trread(a1);
  return __builtin_shufflevector(lo, hi, 0, 1, 2, 3, 4, 5, 6, 7);
}
DI bf16x8 ldsread16(unsigned addr) { return *(LDSP(bf16x8))(size_t)addr; }
DI void ldswrite16(unsigned addr, bf16x8 v) { *(LDSP(bf16x8))(size_t)addr = v; }

template <int OFF> DI void gload(bf16x8& dst, unsigned voff, const void* sbase) {
  asm volatile("global_load_dwordx4 %0, %1, %2 offset:%3" : "=v"(dst) : "v"(voff), "s"(sbase), "i"(OFF) : "memory");
}
DI void dma2(unsigned m0val, unsigned voff, const void* sbase) {
  unsigned keep;
  asm volatile("s_mov_b32 %0, m0\n\ts_mov_b32 m0, %1\n\ts_nop 0\n\t"
               "global_load_lds_dwordx4 %2, %3 nt\n\tglobal_load_lds_dwordx4 %2, %3 offset:1024 nt\n\t"
               "s_mov_b32 m0, %0"
               : "=&s"(keep) : "s"(m0val), "v"(voff), "s"(sbase) : "memory");
}
DI void dma1(unsigned m0val, unsigned voff, const void* sbase) {
  unsigned keep;
  asm volatile("s_mov_b32 %0, m0\n\ts_mov_b32 m0, %1\n\ts_nop 0\n\t"
               "global_load_lds_dwordx4 %2, %3\n\t"
               "s_mov_b32 m0, %0"
               : "=&s"(keep) : "s"(m0val), "v"(voff), "s"(sbase) : "memory");
}
#define STAMP_DECL
#define STAMP(i)
#define RSTAMP(i)
#define STAMP_FLUSH
#define BARRIER() asm volatile("s_waitcnt lgkmcnt(0)\n\ts_barrier" ::: "memory")
template <int N> DI void waitv() { asm volatile("s_waitcnt vmcnt(%0)" ::"i"(N) : "memory"); }
template <int N> DI void waitv2(bf16x8& a, bf16x8& b) { asm volatile("s_waitcnt vmcnt(%2)" : "+v"(a), "+v"(b) : "i"(N) : "memory"); }
template <int N> DI void waitv4(bf16x8& a, bf16x8& b, bf16x8& c, bf16x8& d) {
  asm volatile("s_waitcnt vmcnt(%4)" : "+v"(a), "+v"(b), "+v"(c), "+v"(d) : "i"(N) : "memory");
}

template <int N> DI void waitv6(bf16x8& a, bf16x8& b, bf16x8& c, bf16x8& d, bf16x8& e, bf16x8& f) {
  asm volatile("s_waitcnt vmcnt(%6)" : "+v"(a), "+v"(b), "+v"(c), "+v"(d), "+v"(e), "+v"(f) : "i"(N) : "memory");
}
template <int N> DI void waitv3(bf16x8& a, bf16x8& b, bf16x8& c) {
  asm volatile("s_waitcnt vmcnt(%3)" : "+v"(a), "+v"(b), "+v"(c) : "i"(N) : "memory");
}
DI void dma4(unsigned m0val, unsigned voff, const void* sbase) {
  unsigned keep;
  asm volatile("s_mov_b32 %0, m0\n\ts_mov_b32 m0, %1\n\ts_nop 0\n\t"
               "global_load_lds_dwordx4 %2, %3\n\tglobal_load_lds_dwordx4 %2, %3 offset:1024\n\t"
               "global_load_lds_dwordx4 %2, %3 offset:2048\n\tglobal_load_lds_dwordx4 %2, %3 offset:3072\n\t"
               "s_mov_b32 m0, %0"
               : "=&s"(keep) : "s"(m0val), "v"(voff), "s"(sbase) : "memory");
}
#define OPAQUE(x) asm volatile("" : "+v"(x))
#define SCHED() __builtin_amdgcn_sched_barrier(0)

typedef __attribute__((ext_vector_type(16))) float f32x16;
DI f32x16 mfma32(bf16x8 a, bf16x8 b, f32x16 c) { return __builtin_amdgcn_mfma_f32_32x32x16_bf16(a, b, c, 0, 0, 0); }
template <int OFF> DI void gload_nt(bf16x8& dst, unsigned voff, const void* sbase) {
  asm volatile("global_load_dwordx4 %0, %1, %2 offset:%3 nt" : "=v"(dst) : "v"(voff), "s"(sbase), "i"(OFF) : "memory");
}
DI void dma1nt(unsigned m0val, unsigned voff, const void* sbase) {
  unsigned keep;
  asm volatile("s_mov_b32 %0, m0\n\ts_mov_b32 m0, %1\n\ts_nop 0\n\t"
               "global_load_lds_dwordx4 %2, %3 nt\n\t"
               "s_mov_b32 m0, %0"
               : "=&s"(keep) : "s"(m0val), "v"(voff), "s"(sbase) : "memory");
}
DI void dma1sc(unsigned m0val, unsigned voff, const void* sbase) {
  unsigned keep;
  asm volatile("s_mov_b32 %0, m0\n\ts_mov_b32 m0, %1\n\ts_nop 0\n\t"
               "global_load_lds_dwordx4 %2, %3 sc1\n\t"
               "s_mov_b32 m0, %0"
               : "=&s"(keep) : "s"(m0val), "v"(voff), "s"(sbase) : "memory");
}
DI f32x4 ldsreadf(unsigned addr) { return *(LDSP(f32x4))(size_t)addr; }
DI void ldswritef(unsigned addr, f32x4 v) { *(LDSP(f32x4))(size_t)addr = v; }
DI f32x4 q4of(const f32x16& v, int i) { return f32x4{v[4 * i], v[4 * i + 1], v[4 * i + 2], v[4 * i + 3]}; }

__host__ __device__ constexpr int vslot(int t) { return t < 2 ? 8 + t : t - 2; }

DI void attn_body(const uint16_t* __restrict__ x, const uint16_t* __restrict__ kc, const uint16_t* __restrict__ vc,
                  const uint16_t* __restrict__ wq, uint16_t* __restrict__ ows, unsigned lds0, int wid, int lane, int b, int h) {
  const int r = lane & 15, g = lane >> 4;
  const int q = lane & 31, hh = lane >> 5;
  const int q4 = r >> 2, p = lane & 3, blk = g & 1;
  const int qg = wid & 3, kh = wid >> 2;
  STAMP_DECL RSTAMP(30); STAMP(0);
  const int rot = (b + 3 * (h >> 3)) % 10;
  const uint16_t* wq_s = wq + h * DH + (size_t)(8 * wid) * 4096;
  const uint16_t* x_s = x + (size_t)(b * SQ + 16 * (wid < 7 ? wid : 6)) * DM;
  const uint16_t* kb = kc + (size_t)(b * NH + h) * SK * DH + 8 * wid * DH;
  const uint16_t* vb = vc + (size_t)(b * NH + h) * SK * DH + 8 * wid * DH;
  auto unitof = [&](int hs) { int u = hs + rot; return u >= 10 ? u - 10 : u; };
  const unsigned w_voff = (unsigned)((g * 4096 + 8 * (r ^ (g << 2))) * 2);
  const unsigned v_voff = (unsigned)((g * DH + 8 * (r ^ (g << 2))) * 2);
  const unsigned k_voff0 = (unsigned)((g * DH + 8 * (r ^ (8 * (wid & 1) + g))) * 2);
  const unsigned k_voff1 = (unsigned)(((4 + g) * DH + 8 * (r ^ (8 * (wid & 1) + 4 + g))) * 2);
  const unsigned x_voff0 = (unsigned)(((lane >> 3) * DM + 8 * ((lane & 7) ^ (lane >> 4))) * 2);
  const unsigned x_voff1 = (unsigned)(((8 + (lane >> 3)) * DM + 8 * ((lane & 7) ^ (4 | (lane >> 4)))) * 2);
  auto dmaU1 = [&](int hs, int j) {
    const unsigned d = lds0 + 2 * (hs % 5) * SLOT + wid * 2048 + (j >> 1) * 1024;
    const int u = unitof(hs);
    if ((j & 1) == 0) dma1nt(d, w_voff, wq_s + (size_t)(64 * u + 4 * (j >> 1)) * 4096);
    else dma1(d + SLOT, (j >> 1) ? x_voff1 : x_voff0, x_s + 64 * u);
  };
  auto dmaU = [&](int hs) { dmaU1(hs, 0); dmaU1(hs, 1); dmaU1(hs, 2); dmaU1(hs, 3); };
  auto dmaK1 = [&](int t, int n) { dma1nt(lds0 + t * SLOT + wid * 2048 + n * 1024, n ? k_voff1 : k_voff0, kb + t * 64 * DH); };
  auto dmaV1 = [&](int t, int n) { dma1nt(lds0 + vslot(t) * SLOT + wid * 2048 + n * 1024, v_voff, vb + (t * 64 + 4 * n) * DH); };
  dmaU(0);
  asm volatile("s_barrier" ::: "memory");
  dmaU(1);
  STAMP(1);
  const unsigned tr0 = lds0 + (unsigned)((8 * hh + q4) * 256 + (q4 << 6) + 32 * blk + 8 * p);
  unsigned trb[4];
  const int xrow = 32 * qg + q;
  const unsigned xb = lds0 + SLOT + (unsigned)(xrow * 128 + ((hh ^ ((xrow >> 1) & 7)) << 4));

  f32x16 qa[2];
#pragma unroll
  for (int c = 0; c < 2; ++c)
#pragma unroll
    for (int i = 0; i < 16; ++i) qa[c][i] = 0.f;
  bf16x8 af[2][8], xf[2][4];
  auto dmaT = [&](int i) {
    if (i < 8) { dmaK1(i, 0); dmaK1(i, 1); } else if (i < 16) { dmaV1(i - 8, 0); dmaV1(i - 8, 1); }
  };
#pragma unroll
  for (int hs = 0; hs < 10; ++hs) {
    if (hs <= 8) waitv<4>(); else waitv<2>();
    if (hs == 0) STAMP(2);
    BARRIER();
    if (hs == 2) STAMP(3); if (hs == 6) STAMP(4);
#pragma unroll
    for (int c = 0; c < 2; ++c) { trb[c] = (tr0 ^ ((2 * kh + c) << 6)) + 2 * (hs % 5) * SLOT; OPAQUE(trb[c]); }
    const unsigned xa = xb + 2 * (hs % 5) * SLOT;
    SCHED();
#pragma unroll
    for (int d = 0; d < 8; ++d) {
      if (hs > 0) qa[d & 1] = mfma32(af[(hs - 1) & 1][d], xf[(hs - 1) & 1][d >> 1], qa[d & 1]);
      SCHED();
      if (d < 4) xf[hs & 1][d] = ldsread16(xa ^ (32 * d));
      if (d < 4) af[hs & 1][d] = trpair(trb[d & 1] + 4096 * (d >> 1), trb[d & 1] + 4096 * (d >> 1) + 1024);
      else if (d < 6) {
        af[hs & 1][2 * d - 4] = trpair(trb[0] + 4096 * (d - 2), trb[0] + 4096 * (d - 2) + 1024);
        af[hs & 1][2 * d - 3] = trpair(trb[1] + 4096 * (d - 2), trb[1] + 4096 * (d - 2) + 1024);
      }
      if ((d & 1) == 1 && hs + 2 < 10) dmaU1(hs + 2, d >> 1);
      if (d < 2 && hs >= 8) dmaK1(hs - 8, d);
      SCHED();
    }
  }
#pragma unroll
  for (int d = 0; d < 8; ++d) qa[d & 1] = mfma32(af[1][d], xf[1][d >> 1], qa[d & 1]);
  dmaT(2);
  constexpr float C = 0.08838834764831845f * 1.4426950408889634f;
  qa[0] *= C; qa[1] *= C;
  bf16x8 qf[8];
  {
    const unsigned mine = lds0 + 6 * SLOT + wid * 4096 + lane * 16, other = lds0 + 6 * SLOT + (wid ^ 4) * 4096 + lane * 16;
    bf16x8 qm[4];
#pragma unroll
    for (int c = 0; c < 2; ++c)
#pragma unroll
      for (int s = 0; s < 2; ++s) {
        unsigned a0 = cvtpk(qa[c][8 * s], qa[c][8 * s + 1]), a1 = cvtpk(qa[c][8 * s + 2], qa[c][8 * s + 3]);
        unsigned b0 = cvtpk(qa[c][8 * s + 4], qa[c][8 * s + 5]), b1 = cvtpk(qa[c][8 * s + 6], qa[c][8 * s + 7]);
        auto s0 = __builtin_amdgcn_permlane32_swap(a0, b0, false, false);
        auto s1 = __builtin_amdgcn_permlane32_swap(a1, b1, false, false);
        u32x4 v = {s0[0], s1[0], s0[1], s1[1]};
        qm[2 * c + s] = __builtin_bit_cast(bf16x8, v);
        ldswrite16(mine + (2 * c + s) * 1024, qm[2 * c + s]);
      }
    BARRIER();
    dmaT(3);
#pragma unroll
    for (int i = 0; i < 4; ++i) {
      const bf16x8 o = ldsread16(other + i * 1024);
      qf[i] = kh ? o : qm[i];
      qf[4 + i] = kh ? qm[i] : o;
    }
  }
  STAMP(5);
  constexpr float THR = 8.0f;
  const int key = (q & 19) | ((q & 4) << 1) | ((q & 8) >> 1);
  const unsigned kr0 = lds0 + kh * 8192 + (unsigned)(key * 256 + ((hh ^ (key & 15)) << 4));
  unsigned krb[8];
  bf16x8 kf[2][8];
  f32x16 sc[2], scn, minit;
#pragma unroll
  for (int i = 0; i < 16; ++i) minit[i] = 0.f;
  bf16x8 pf[8][2];
  float alpha[8];
  unsigned resc = 0;
  float mref = 0.f, off = 0.f;
  bool offf = false;
  auto sm_max = [&](int t) {
    f32x16& v = sc[t & 1];
    float m0 = fmaxf(fmaxf(v[0], v[1]), v[2]), m1 = fmaxf(fmaxf(v[3], v[4]), v[5]);
    m0 = fmaxf(fmaxf(m0, v[6]), v[7]); m1 = fmaxf(fmaxf(m1, v[8]), v[9]);
    m0 = fmaxf(fmaxf(m0, v[10]), v[11]); m1 = fmaxf(fmaxf(m1, v[12]), v[13]);
    m0 = fmaxf(fmaxf(m0, v[14]), v[15]);
    m0 = fmaxf(m0, m1);
    auto sw = __builtin_amdgcn_permlane32_swap(__float_as_uint(m0), __float_as_uint(m0), false, false);
    m0 = fmaxf(__uint_as_float(sw[0]), __uint_as_float(sw[1]));
    alpha[t] = 1.f;
    m0 -= off;
    if (t <= 1 || __builtin_expect(offf || __any(m0 > THR), 0)) {
      const bool upd = t == 0 || __any(m0 > THR);
      const float dl = t == 0 ? m0 : (upd ? fmaxf(m0, 0.f) : 0.f);
      if (t > 0 && upd) { alpha[t] = __builtin_amdgcn_exp2f(-dl); resc |= 1u << t; }
      mref += dl;
      v -= off + dl;
      off = dl; offf = upd;
#pragma unroll
      for (int i = 0; i < 16; ++i) minit[i] = -mref;
    }
  };
  unsigned pw[8][8];
  float sh[8][8];
  auto sm_exp = [&](int t, int j) {
    float e[4];
#pragma unroll
    for (int i = 0; i < 4; ++i) e[i] = __builtin_amdgcn_exp2f(sc[t & 1][4 * j + i]);
    pw[t][2 * j] = cvtpk(e[0], e[1]);
    pw[t][2 * j + 1] = cvtpk(e[2], e[3]);
    asm volatile("" : "+v"(pw[t][2 * j]), "+v"(pw[t][2 * j + 1]));
  };
#pragma unroll
  for (int t = 0; t < 10; ++t) {
    if (t < 8) {
      waitv<6>();
      BARRIER();
      STAMP(6 + t);
      if ((t & 3) == 0) {
#pragma unroll
        for (int s = 0; s < 8; ++s) { krb[s] = (kr0 ^ (s << 5)) + t * SLOT; OPAQUE(krb[s]); }
      }
    }
    SCHED();
#pragma unroll
    for (int s = 0; s < 8; ++s) {
      if (t >= 1 && t <= 8) {
        if (s == 0) scn = mfma32(kf[(t - 1) & 1][0], qf[0], minit);
        else scn = mfma32(kf[(t - 1) & 1][s], qf[s], scn);
      }
      SCHED();
      if (t < 8 && s < 4) {
        kf[t & 1][2 * s] = ldsread16(krb[2 * s] + (t & 3) * SLOT);
        kf[t & 1][2 * s + 1] = ldsread16(krb[2 * s + 1] + (t & 3) * SLOT);
      }
      if (t >= 2 && s == 1) sm_max(t - 2);
      if (t >= 2 && (s == 3 || s == 5)) sm_exp(t - 2, (s - 3) >> 1);
      if (t >= 2 && s == 2) {
#pragma unroll
        for (int i = 0; i < 8; ++i) sh[t - 2][i] = sc[t & 1][8 + i];
      }
      SCHED();
    }
    if (t >= 1 && t <= 8) sc[(t - 1) & 1] = scn;
    if (t < 8) dmaT(4 + t);
  }
#pragma unroll
  for (int t = 0; t < 8; ++t) {
    u32x4 v = {pw[t][0], pw[t][1], pw[t][2], pw[t][3]};
    pf[t][0] = __builtin_bit_cast(bf16x8, v);
  }
  auto sm_exp2 = [&](int t, int j) {
    float e[4];
#pragma unroll
    for (int i = 0; i < 4; ++i) e[i] = __builtin_amdgcn_exp2f(sh[t][4 * j + i]);
    pw[t][4 + 2 * j] = cvtpk(e[0], e[1]);
    pw[t][5 + 2 * j] = cvtpk(e[2], e[3]);
    asm volatile("" : "+v"(pw[t][4 + 2 * j]), "+v"(pw[t][5 + 2 * j]));
    if (j == 1) {
      u32x4 v = {pw[t][4], pw[t][5], pw[t][6], pw[t][7]};
      pf[t][1] = __builtin_bit_cast(bf16x8, v);
    }
  };
  STAMP(14);
  f32x16 oa[4];
#pragma unroll
  for (int c = 0; c < 4; ++c)
#pragma unroll
    for (int i = 0; i < 16; ++i) oa[c][i] = 0.f;
  f32x16 la;
#pragma unroll
  for (int i = 0; i < 16; ++i) la[i] = 0.f;
  const bf16x8 ones = {0x3F80, 0x3F80, 0x3F80, 0x3F80, 0x3F80, 0x3F80, 0x3F80, 0x3F80};
  const int cs = 2 * (kh ^ 1);
#pragma unroll
  for (int t = 0; t < 8; ++t) {
    switch (t) {
      case 0: case 1: case 2: case 3: case 4: waitv<6>(); break;
      case 5: waitv<4>(); break;
      case 6: waitv<2>(); break;
      case 7: waitv<0>(); break;
    }
    BARRIER();
    STAMP(16 + t);
    if (t == 0 || t == 2 || t == 6) {
#pragma unroll
      for (int c = 0; c < 4; ++c) { trb[c] = (tr0 ^ ((c ^ cs) << 6)) + kh * 8192 + (t == 0 ? 8 * SLOT : t == 2 ? 0 : 4 * SLOT); OPAQUE(trb[c]); }
    }
    const unsigned so = (t < 2 ? t : t < 6 ? t - 2 : t - 6) * SLOT;
    if (t > 1) {
      if (__builtin_expect((resc >> (t - 1)) & 1, 0)) {
#pragma unroll
        for (int c = 0; c < 4; ++c) oa[c] *= alpha[t - 1];
        la *= alpha[t - 1];
      }
    }
    SCHED();
#pragma unroll
    for (int i = 0; i < 8; ++i) {
      if (t > 0) oa[i >> 1] = mfma32(af[(t - 1) & 1][i], pf[t - 1][i & 1], oa[i >> 1]);
      SCHED();
      if (i < 4) af[t & 1][i] = trpair(trb[i >> 1] + so + 4096 * (i & 1), trb[i >> 1] + so + 4096 * (i & 1) + 1024);
      else if (i < 6) {
        af[t & 1][2 * i - 4] = trpair(trb[i - 2] + so, trb[i - 2] + so + 1024);
        af[t & 1][2 * i - 3] = trpair(trb[i - 2] + so + 4096, trb[i - 2] + so + 4096 + 1024);
      }
      if (i == 2 || i == 6) sm_exp2(t, i >> 2);
      SCHED();
    }
    if (t > 0) { la = mfma32(ones, pf[t - 1][0], la); la = mfma32(ones, pf[t - 1][1], la); }
    SCHED();
    dmaT(12 + t);
  }
  if (__builtin_expect((resc >> 7) & 1, 0)) {
#pragma unroll
    for (int c = 0; c < 4; ++c) oa[c] *= alpha[7];
    la *= alpha[7];
  }
  la = mfma32(ones, pf[7][0], la); la = mfma32(ones, pf[7][1], la);
#pragma unroll
  for (int i = 0; i < 4; ++i) oa[i >> 1] = mfma32(af[1][i], pf[7][i & 1], oa[i >> 1]);
  STAMP(24);
  {
    const float l = la[0];
    const unsigned mine = lds0 + 6 * SLOT + wid * 4096 + lane * 16, other = lds0 + 6 * SLOT + (wid ^ 4) * 4096 + lane * 16;
    const unsigned mlm = lds0 + wid * 256 + q * 8, mlo = lds0 + (wid ^ 4) * 256 + q * 8;
    *(LDSP(f32x2))(size_t)mlm = f32x2{mref, l};
#pragma unroll
    for (int i = 0; i < 4; ++i) oa[2 + (i >> 1)] = mfma32(af[1][4 + i], pf[7][i & 1], oa[2 + (i >> 1)]);
#pragma unroll
    for (int c = 0; c < 2; ++c)
#pragma unroll
      for (int hf = 0; hf < 2; ++hf) {
        u32x4 v = {cvtpk(oa[c][8 * hf], oa[c][8 * hf + 1]), cvtpk(oa[c][8 * hf + 2], oa[c][8 * hf + 3]),
                   cvtpk(oa[c][8 * hf + 4], oa[c][8 * hf + 5]), cvtpk(oa[c][8 * hf + 6], oa[c][8 * hf + 7])};
        ldswrite16(mine + (2 * c + hf) * 1024, __builtin_bit_cast(bf16x8, v));
      }
    BARRIER();
    const f32x2 ml = *(LDSP(f32x2))(size_t)mlo;
    u32x4 pv[4];
#pragma unroll
    for (int i = 0; i < 4; ++i) pv[i] = __builtin_bit_cast(u32x4, ldsread16(other + i * 1024));
    const float m = fmaxf(mref, ml[0]);
    const float f0 = __builtin_amdgcn_exp2f(mref - m), f1 = __builtin_amdgcn_exp2f(ml[0] - m);
    const float linv = __builtin_amdgcn_rcpf(l * f0 + ml[1] * f1);
    const float s0 = f0 * linv, s1 = f1 * linv;
    uint16_t* op = ows + ((size_t)((b * 7 + 2 * qg + (q >> 4)) * 128 + h * 4 + 2 * kh) * 512) + (hh * 16 + (q & 15)) * 8;
    if (xrow < SQ) {
#pragma unroll
      for (int c = 0; c < 2; ++c)
#pragma unroll
        for (int hf = 0; hf < 2; ++hf) {
          u32x4 v;
#pragma unroll
          for (int i = 0; i < 4; ++i) {
            const unsigned w = pv[2 * c + hf][i];
            const float lo = fmaf(oa[2 + c][8 * hf + 2 * i], s0, __uint_as_float(w << 16) * s1);
            const float hi = fmaf(oa[2 + c][8 * hf + 2 * i + 1], s0, __uint_as_float(w & 0xffff0000u) * s1);
            v[i] = cvtpk(lo, hi);
          }
          asm volatile("global_store_dwordx4 %0, %1, off sc1\n\ts_nop 1" :: "v"(op + c * 512 + hf * 256), "v"(v) : "memory");
        }
    }
  }
  STAMP(25); RSTAMP(31);
  STAMP_FLUSH;
}

extern "C" __device__ unsigned long long amd_dispatch_id() __asm("llvm.amdgcn.dispatch.id");
typedef unsigned long long u64;
constexpr int KSLOT = 13312;
constexpr size_t WS_FO = (size_t)8 << 20;
constexpr size_t WS_FX = WS_FO + 65536;
constexpr size_t WS_X = (size_t)9 << 20;
template <bool LOADER>
DI void oproj_body(const uint16_t* __restrict__ ows, const uint16_t* __restrict__ wo, uint16_t* __restrict__ out, char* __restrict__ ws,
                   unsigned lds0, int wid, int lane, u64 tag) {
  const int xcd = blockIdx.x & 7, idx = blockIdx.x >> 3;
  const int kq = xcd >> 1, mh = xcd & 1, mgi = idx / 5, nb = idx % 5;
  const int mt0 = 28 * mh + (mgi < 4 ? 5 * mgi : 20 + 4 * (mgi - 4)), mcnt = mgi < 4 ? 5 : 4;
  const int r = lane & 15, g = lane >> 4, q4 = r >> 2, p = lane & 3;
  const int rot = (mgi + nb) & 7;
  const unsigned o_voff = (unsigned)lane * 16;
  if constexpr (LOADER) {
    const int lj = wid - 4;
    const unsigned w_voff0 = (unsigned)((g * DM + 8 * (r ^ (g << 1))) * 2), w_voff1 = (unsigned)((g * DM + 8 * (r ^ ((4 | g) << 1))) * 2);
    const uint16_t* wo_s = wo + (size_t)(1024 * kq + 32 * lj) * DM + 128 * nb;
    const uint16_t* o_s = ows + ((size_t)mt0 * 128 + 32 * kq + lj) * 512;
    auto issue = [&](int u, bool dw, bool dz) {
      const int uu = (u + rot) & 7;
      const unsigned d = lds0 + ((u % 3) * 4 + lj) * KSLOT;
#pragma unroll
      for (int i = 0; i < 8; ++i) {
        if (dw) dma1(d + i * 1024, (i & 1) ? w_voff1 : w_voff0, wo_s + (size_t)(128 * uu + 4 * i) * DM);
        if (dz && i < 5) dma1sc(d + 8192 + i * 1024, o_voff, o_s + ((size_t)(i < mcnt ? i : mcnt - 1) * 128 + 4 * uu) * 512);
      }
    };
    issue(0, true, false); issue(1, true, false); issue(2, true, false);
    BARRIER();
    issue(0, false, true); issue(1, false, true); issue(2, false, true);
#pragma unroll
    for (int u = 0; u < 8; ++u) {
      if (u == 0) waitv<10>(); else if (u == 1) waitv<5>(); else if (u < 7) waitv<13>(); else waitv<0>();
      BARRIER();
      if (u >= 1 && u + 2 < 8) issue(u + 2, true, true);
    }
    return;
  } else {
  const int np = wid;
  const int trow = 16 * (g >> 1) + 4 * (g & 1) + q4;
  const unsigned tra = lds0 + (unsigned)(trow * 256 + (((2 * np) ^ (trow & 7)) << 5) + 8 * p);
  const unsigned ofa = lds0 + 8192 + lane * 16;
  f32x4 acc[2][5];
#pragma unroll
  for (int c = 0; c < 2; ++c)
#pragma unroll
    for (int m = 0; m < 5; ++m) acc[c][m] = f32x4{0.f, 0.f, 0.f, 0.f};
  bf16x8 af[2][2], bf[2][5];
  if (wid == 0) {
    const int bb = (lane & 8) ? (mt0 + mcnt - 1) / 7 : mt0 / 7;
    u64* f = (u64*)(ws + WS_FO) + bb * 32 + 8 * kq + (lane & 7);
    for (int spin = 0; spin < (1 << 22); ++spin) {
      if (__all(__hip_atomic_load(f, __ATOMIC_RELAXED, __HIP_MEMORY_SCOPE_AGENT) == tag)) break;
      __builtin_amdgcn_s_sleep(1);
    }
  }
  BARRIER();
#pragma unroll
  for (int u = 0; u < 8; ++u) {
    BARRIER();
    const unsigned ub = (u % 3) * 4 * KSLOT;
    unsigned t0 = tra + ub, t1 = (tra ^ 32) + ub, ob = ofa + ub;
    OPAQUE(t0); OPAQUE(t1); OPAQUE(ob);
    SCHED();
#pragma unroll
    for (int j = 0; j < 4; ++j) {
#pragma unroll
      for (int m = 0; m < 5; ++m) {
        if (u > 0 || j > 0) {
          acc[0][m] = mfma16(af[(j + 1) & 1][0], bf[(j + 1) & 1][m], acc[0][m]);
          acc[1][m] = mfma16(af[(j + 1) & 1][1], bf[(j + 1) & 1][m], acc[1][m]);
        }
        SCHED();
        if (m == 0) af[j & 1][0] = trpair(t0 + j * KSLOT, t0 + j * KSLOT + 2048);
        if (m == 1) af[j & 1][1] = trpair(t1 + j * KSLOT, t1 + j * KSLOT + 2048);
        if (m < 2) bf[j & 1][m] = ldsread16(ob + j * KSLOT + m * 1024);
        if (m == 2) { bf[j & 1][2] = ldsread16(ob + j * KSLOT + 2048); bf[j & 1][3] = ldsread16(ob + j * KSLOT + 3072); bf[j & 1][4] = ldsread16(ob + j * KSLOT + 4096); }
        SCHED();
      }
    }
  }
#pragma unroll
  for (int m = 0; m < 5; ++m) {
    acc[0][m] = mfma16(af[1][0], bf[1][m], acc[0][m]);
    acc[1][m] = mfma16(af[1][1], bf[1][m], acc[1][m]);
  }
  const int dest = (idx << 3) | (np << 1) | mh;
  if (np != kq) {
    char* xp = ws + WS_X + ((size_t)(dest * 4 + kq) * 5) * 1024;
#pragma unroll
    for (int m = 0; m < 5; ++m) {
      u32x4 v = {cvtpk(acc[0][m][0], acc[0][m][1]), cvtpk(acc[0][m][2], acc[0][m][3]), cvtpk(acc[1][m][0], acc[1][m][1]), cvtpk(acc[1][m][2], acc[1][m][3])};
      asm volatile("global_store_dwordx4 %0, %1, %2 offset:0 sc1\n\ts_nop 1" :: "v"(o_voff + (unsigned)(m * 1024)), "v"(v), "s"(xp) : "memory");
    }
    asm volatile("s_waitcnt vmcnt(0)" ::: "memory");
    if (lane == 0) __hip_atomic_store((u64*)(ws + WS_FX + (size_t)dest * 128) + kq, tag, __ATOMIC_RELAXED, __HIP_MEMORY_SCOPE_AGENT);
    return;
  }
  {
    u64* f = (u64*)(ws + WS_FX + (size_t)blockIdx.x * 128) + ((kq + 1 + (lane % 3)) & 3);
    for (int spin = 0; spin < (1 << 22); ++spin) {
      if (__all(__hip_atomic_load(f, __ATOMIC_RELAXED, __HIP_MEMORY_SCOPE_AGENT) == tag)) break;
      __builtin_amdgcn_s_sleep(1);
    }
    asm volatile("s_barrier" ::: "memory");
    const char* xp = ws + WS_X + ((size_t)(blockIdx.x * 4) * 5) * 1024;
    u32x4 v[3][5];
#pragma unroll
    for (int s = 0; s < 3; ++s)
#pragma unroll
      for (int m = 0; m < 5; ++m)
        asm volatile("global_load_dwordx4 %0, %1, %2 offset:0 sc1" : "=v"(v[s][m]) : "v"(o_voff + (unsigned)(m * 1024)), "s"(xp + (size_t)((kq + s + 1) & 3) * 5120) : "memory");
    asm volatile("s_waitcnt vmcnt(0)" : "+v"(v[0][0]), "+v"(v[0][1]), "+v"(v[0][2]), "+v"(v[0][3]), "+v"(v[0][4]),
                 "+v"(v[1][0]), "+v"(v[1][1]), "+v"(v[1][2]), "+v"(v[1][3]), "+v"(v[1][4]),
                 "+v"(v[2][0]), "+v"(v[2][1]), "+v"(v[2][2]), "+v"(v[2][3]), "+v"(v[2][4]) :: "memory");
#pragma unroll
    for (int m = 0; m < 5; ++m)
#pragma unroll
      for (int c = 0; c < 2; ++c) {
        f32x4 t = acc[c][m];
#pragma unroll
        for (int s = 0; s < 3; ++s) {
          const unsigned w0 = v[s][m][2 * c], w1 = v[s][m][2 * c + 1];
          t += f32x4{__uint_as_float(w0 << 16), __uint_as_float(w0 & 0xffff0000u), __uint_as_float(w1 << 16), __uint_as_float(w1 & 0xffff0000u)};
        }
        if (m < mcnt) {
          u32x2 o = {cvtpk(t[0], t[1]), cvtpk(t[2], t[3])};
          *(u32x2*)(out + (size_t)((mt0 + m) * 16 + r) * DM + 128 * nb + 16 * (2 * np + c) + 4 * g) = o;
        }
      }
  }
  }
}
__global__ __launch_bounds__(512, 2) void fused_kernel(const uint16_t* __restrict__ x, const uint16_t* __restrict__ kc,
                                                       const uint16_t* __restrict__ vc, const uint16_t* __restrict__ wq,
                                                       const uint16_t* __restrict__ wo, uint16_t* __restrict__ out, char* __restrict__ ws) {
  extern __shared__ __attribute__((aligned(16))) char smem[];
  const int lane = threadIdx.x & 63;
  const int wid = __builtin_amdgcn_readfirstlane(threadIdx.x >> 6);
  const int h = blockIdx.x & 31, b = blockIdx.x >> 5;
  const unsigned lds0 = (unsigned)(size_t)smem;
  const u64 tag = (amd_dispatch_id() + 1) * 0x9E3779B97F4A7C15ull + (u64)(uintptr_t)__builtin_amdgcn_dispatch_ptr() * 0xC2B2AE3D27D4EB4Full + 0x165667B19E3779F9ull;
  attn_body(x, kc, vc, wq, (uint16_t*)ws, lds0, wid, lane, b, h);
  asm volatile("s_waitcnt vmcnt(0)" ::: "memory");
  BARRIER();
  if (threadIdx.x == 0) __hip_atomic_store((u64*)(ws + WS_FO) + blockIdx.x, tag, __ATOMIC_RELAXED, __HIP_MEMORY_SCOPE_AGENT);
  if (blockIdx.x >= 240) return;
  if (wid < 4) oproj_body<false>((const uint16_t*)ws, wo, out, ws, lds0, wid, lane, tag);
  else oproj_body<true>((const uint16_t*)ws, wo, out, ws, lds0, wid, lane, tag);
}

extern "C" void kernel_launch(void* const* d_in, const int* in_sizes, int n_in, void* d_out, int out_size, void* d_ws,
                              size_t ws_size, hipStream_t stream) {
  const uint16_t* x = (const uint16_t*)d_in[0];
  const uint16_t* kc = (const uint16_t*)d_in[1];
  const uint16_t* vc = (const uint16_t*)d_in[2];
  const uint16_t* wq = (const uint16_t*)d_in[3];
  const uint16_t* wo = (const uint16_t*)d_in[4];
  (void)hipFuncSetAttribute((const void*)fused_kernel, hipFuncAttributeMaxDynamicSharedMemorySize, 10 * SLOT);
  fused_kernel<<<256, 512, 10 * SLOT, stream>>>(x, kc, vc, wq, wo, (uint16_t*)d_out, (char*)d_ws);
}
```

```cpp
#include <hip/hip_runtime.h>
#include <hip/hip_bf16.h>
#include <stdint.h>

typedef __attribute__((ext_vector_type(8))) short bf16x8;
typedef __attribute__((ext_vector_type(4))) short s16x4;
typedef __attribute__((ext_vector_type(4))) float f32x4;
typedef __attribute__((ext_vector_type(4))) unsigned u32x4;
typedef __attribute__((ext_vector_type(2))) unsigned u32x2;
#define LDSP(T) __attribute__((address_space(3))) T*
#define DI __device__ __forceinline__

constexpr int NB = 8, SQ = 112, DM = 640, NH = 32, SK = 512, DH = 128;
constexpr int SLOT = 16384;

typedef __attribute__((ext_vector_type(2))) float f32x2;
typedef __attribute__((ext_vector_type(2))) __bf16 bf16x2;
DI unsigned cvtpk(float lo, float hi) {
  f32x2 v = {lo, hi};
  return __builtin_bit_cast(unsigned, __builtin_convertvector(v, bf16x2));
}
DI f32x4 mfma16(bf16x8 a, bf16x8 b, f32x4 c) { return __builtin_amdgcn_mfma_f32_16x16x32_bf16(a, b, c, 0, 0, 0); }
DI s16x4 trread(unsigned addr) { return __builtin_amdgcn_ds_read_tr16_b64_v4i16((LDSP(s16x4))(size_t)addr); }
DI bf16x8 trpair(unsigned a0, unsigned a1) {
  s16x4 lo = trread(a0), hi = trread(a1);
  return __builtin_shufflevector(lo, hi, 0, 1, 2, 3, 4, 5, 6, 7);
}
DI bf16x8 ldsread16(unsigned addr) { return *(LDSP(bf16x8))(size_t)addr; }
DI void ldswrite16(unsigned addr, bf16x8 v) { *(LDSP(bf16x8))(size_t)addr = v; }

template <int OFF> DI void gload(bf16x8& dst, unsigned voff, const void* sbase) {
  asm volatile("global_load_dwordx4 %0, %1, %2 offset:%3" : "=v"(dst) : "v"(voff), "s"(sbase), "i"(OFF) : "memory");
}
DI void dma2(unsigned m0val, unsigned voff, const void* sbase) {
  unsigned keep;
  asm volatile("s_mov_b32 %0, m0\n\ts_mov_b32 m0, %1\n\ts_nop 0\n\t"
               "global_load_lds_dwordx4 %2, %3 nt\n\tglobal_load_lds_dwordx4 %2, %3 offset:1024 nt\n\t"
               "s_mov_b32 m0, %0"
               : "=&s"(keep) : "s"(m0val), "v"(voff), "s"(sbase) : "memory");
}
DI void dma1(unsigned m0val, unsigned voff, const void* sbase) {
  unsigned keep;
  asm volatile("s_mov_b32 %0, m0\n\ts_mov_b32 m0, %1\n\ts_nop 0\n\t"
               "global_load_lds_dwordx4 %2, %3\n\t"
               "s_mov_b32 m0, %0"
               : "=&s"(keep) : "s"(m0val), "v"(voff), "s"(sbase) : "memory");
}
#define STAMP_DECL
#define STAMP(i)
#define RSTAMP(i)
#define STAMP_FLUSH
#define BARRIER() asm volatile("s_waitcnt lgkmcnt(0)\n\ts_barrier" ::: "memory")
template <int N> DI void waitv() { asm volatile("s_waitcnt vmcnt(%0)" ::"i"(N) : "memory"); }
template <int N> DI void waitv2(bf16x8& a, bf16x8& b) { asm volatile("s_waitcnt vmcnt(%2)" : "+v"(a), "+v"(b) : "i"(N) : "memory"); }
template <int N> DI void waitv4(bf16x8& a, bf16x8& b, bf16x8& c, bf16x8& d) {
  asm volatile("s_waitcnt vmcnt(%4)" : "+v"(a), "+v"(b), "+v"(c), "+v"(d) : "i"(N) : "memory");
}

template <int N> DI void waitv6(bf16x8& a, bf16x8& b, bf16x8& c, bf16x8& d, bf16x8& e, bf16x8& f) {
  asm volatile("s_waitcnt vmcnt(%6)" : "+v"(a), "+v"(b), "+v"(c), "+v"(d), "+v"(e), "+v"(f) : "i"(N) : "memory");
}
template <int N> DI void waitv3(bf16x8& a, bf16x8& b, bf16x8& c) {
  asm volatile("s_waitcnt vmcnt(%3)" : "+v"(a), "+v"(b), "+v"(c) : "i"(N) : "memory");
}
DI void dma4(unsigned m0val, unsigned voff, const void* sbase) {
  unsigned keep;
  asm volatile("s_mov_b32 %0, m0\n\ts_mov_b32 m0, %1\n\ts_nop 0\n\t"
               "global_load_lds_dwordx4 %2, %3\n\tglobal_load_lds_dwordx4 %2, %3 offset:1024\n\t"
               "global_load_lds_dwordx4 %2, %3 offset:2048\n\tglobal_load_lds_dwordx4 %2, %3 offset:3072\n\t"
               "s_mov_b32 m0, %0"
               : "=&s"(keep) : "s"(m0val), "v"(voff), "s"(sbase) : "memory");
}
#define OPAQUE(x) asm volatile("" : "+v"(x))
#define SCHED() __builtin_amdgcn_sched_barrier(0)

typedef __attribute__((ext_vector_type(16))) float f32x16;
DI f32x16 mfma32(bf16x8 a, bf16x8 b, f32x16 c) { return __builtin_amdgcn_mfma_f32_32x32x16_bf16(a, b, c, 0, 0, 0); }
template <int OFF> DI void gload_nt(bf16x8& dst, unsigned voff, const void* sbase) {
  asm volatile("global_load_dwordx4 %0, %1, %2 offset:%3 nt" : "=v"(dst) : "v"(voff), "s"(sbase), "i"(OFF) : "memory");
}
DI void dma1nt(unsigned m0val, unsigned voff, const void* sbase) {
  unsigned keep;
  asm volatile("s_mov_b32 %0, m0\n\ts_mov_b32 m0, %1\n\ts_nop 0\n\t"
               "global_load_lds_dwordx4 %2, %3 nt\n\t"
               "s_mov_b32 m0, %0"
               : "=&s"(keep) : "s"(m0val), "v"(voff), "s"(sbase) : "memory");
}
DI void dma1sc(unsigned m0val, unsigned voff, const void* sbase) {
  unsigned keep;
  asm volatile("s_mov_b32 %0, m0\n\ts_mov_b32 m0, %1\n\ts_nop 0\n\t"
               "global_load_lds_dwordx4 %2, %3 sc1\n\t"
               "s_mov_b32 m0, %0"
               : "=&s"(keep) : "s"(m0val), "v"(voff), "s"(sbase) : "memory");
}
DI f32x4 ldsreadf(unsigned addr) { return *(LDSP(f32x4))(size_t)addr; }
DI void ldswritef(unsigned addr, f32x4 v) { *(LDSP(f32x4))(size_t)addr = v; }
DI f32x4 q4of(const f32x16& v, int i) { return f32x4{v[4 * i], v[4 * i + 1], v[4 * i + 2], v[4 * i + 3]}; }

__host__ __device__ constexpr int vslot(int t) { return t < 2 ? 8 + t : t - 2; }

constexpr int WSL = 10240, OSLOT = 7168, P2_C = 131072;
__host__ __device__ constexpr int wslot2(int ks) { return ks < 6 ? ks * WSL : ks < 12 ? 65536 + (ks - 6) * WSL : ks < 14 ? (ks - 8) * WSL : 65536 + (ks - 14) * WSL; }
struct WoOff { unsigned own, p8, p9; };
DI unsigned wo_voff(int pc, int lane) {
  const int id = 64 * pc + lane, row = id / 20, pos = id % 20;
  return (unsigned)((row * DM + 8 * (pos ^ (((row >> 2) & 1) << 1))) * 2);
}
DI void dmaWo1(const uint16_t* __restrict__ wo, unsigned lds0, int h, int ks, int slot, int pc, unsigned voff) {
  const int head = (h & 7) + 8 * (((h >> 3) + (ks >> 2)) & 3);
  dma1(lds0 + slot + pc * 1024, voff, wo + (size_t)(128 * head + 32 * (ks & 3)) * DM + 160 * (h >> 3));
}
DI void dmaWo(const uint16_t* __restrict__ wo, unsigned lds0, int wid, const WoOff& wv, int h, int grp, int i0 = 0, int i1 = 8) {
#pragma unroll
  for (int i = i0; i < i1; ++i) {
    if (grp < 2) {
      if (i < 6) dmaWo1(wo, lds0, h, 6 * grp + i, wslot2(6 * grp + i), wid, wv.own);
      else {
        const int kl = wid < 6 ? wid : 5;
        dmaWo1(wo, lds0, h, 6 * grp + kl, wslot2(6 * grp) + kl * WSL, i + 2, i == 6 ? wv.p8 : wv.p9);
      }
    } else {
      if (i < 4) dmaWo1(wo, lds0, h, 12 + i, wslot2(12 + i), wid, wv.own);
      else if (i == 4) {
        const int kl = wid >> 1;
        dmaWo1(wo, lds0, h, 12 + kl, (kl < 2 ? wslot2(12) : wslot2(14) - 2 * WSL) + kl * WSL, 8 + (wid & 1), (wid & 1) ? wv.p9 : wv.p8);
      }
    }
  }
}

DI void attn_body(const uint16_t* __restrict__ x, const uint16_t* __restrict__ kc, const uint16_t* __restrict__ vc,
                  const uint16_t* __restrict__ wq, const uint16_t* __restrict__ wo, uint16_t* __restrict__ ows, unsigned lds0, int wid, int lane, int b, int h, WoOff& wv) {
  const int r = lane & 15, g = lane >> 4;
  const int q = lane & 31, hh = lane >> 5;
  const int q4 = r >> 2, p = lane & 3, blk = g & 1;
  const int qg = wid & 3, kh = wid >> 2;
  STAMP_DECL RSTAMP(30); STAMP(0);
  const int rot = (b + 3 * (h >> 3)) % 10;
  const uint16_t* wq_s = wq + h * DH + (size_t)(8 * wid) * 4096;
  const uint16_t* x_s = x + (size_t)(b * SQ + 16 * (wid < 7 ? wid : 6)) * DM;
  const uint16_t* kb = kc + (size_t)(b * NH + h) * SK * DH + 8 * wid * DH;
  const uint16_t* vb = vc + (size_t)(b * NH + h) * SK * DH + 8 * wid * DH;
  auto unitof = [&](int hs) { int u = hs + rot; return u >= 10 ? u - 10 : u; };
  const unsigned w_voff = (unsigned)((g * 4096 + 8 * (r ^ (g << 2))) * 2);
  const unsigned v_voff = (unsigned)((g * DH + 8 * (r ^ (g << 2))) * 2);
  const unsigned k_voff0 = (unsigned)((g * DH + 8 * (r ^ (8 * (wid & 1) + g))) * 2);
  const unsigned k_voff1 = (unsigned)(((4 + g) * DH + 8 * (r ^ (8 * (wid & 1) + 4 + g))) * 2);
  const unsigned x_voff0 = (unsigned)(((lane >> 3) * DM + 8 * ((lane & 7) ^ (lane >> 4))) * 2);
  const unsigned x_voff1 = (unsigned)(((8 + (lane >> 3)) * DM + 8 * ((lane & 7) ^ (4 | (lane >> 4)))) * 2);
  auto dmaU1 = [&](int hs, int j) {
    const unsigned d = lds0 + 2 * (hs % 5) * SLOT + wid * 2048 + (j >> 1) * 1024;
    const int u = unitof(hs);
    if ((j & 1) == 0) dma1nt(d, w_voff, wq_s + (size_t)(64 * u + 4 * (j >> 1)) * 4096);
    else dma1(d + SLOT, (j >> 1) ? x_voff1 : x_voff0, x_s + 64 * u);
  };
  auto dmaU = [&](int hs) { dmaU1(hs, 0); dmaU1(hs, 1); dmaU1(hs, 2); dmaU1(hs, 3); };
  auto dmaK1 = [&](int t, int n) { dma1nt(lds0 + t * SLOT + wid * 2048 + n * 1024, n ? k_voff1 : k_voff0, kb + t * 64 * DH); };
  auto dmaV1 = [&](int t, int n) { dma1nt(lds0 + vslot(t) * SLOT + wid * 2048 + n * 1024, v_voff, vb + (t * 64 + 4 * n) * DH); };
  dmaU(0);
  asm volatile("s_barrier" ::: "memory");
  dmaU(1);
  STAMP(1);
  const unsigned tr0 = lds0 + (unsigned)((8 * hh + q4) * 256 + (q4 << 6) + 32 * blk + 8 * p);
  unsigned trb[4];
  const int xrow = 32 * qg + q;
  const unsigned xb = lds0 + SLOT + (unsigned)(xrow * 128 + ((hh ^ ((xrow >> 1) & 7)) << 4));

  f32x16 qa[2];
#pragma unroll
  for (int c = 0; c < 2; ++c)
#pragma unroll
    for (int i = 0; i < 16; ++i) qa[c][i] = 0.f;
  bf16x8 af[2][8], xf[2][4];
  auto dmaT = [&](int i) {
    if (i < 8) { dmaK1(i, 0); dmaK1(i, 1); } else if (i < 16) { dmaV1(i - 8, 0); dmaV1(i - 8, 1); }
  };
#pragma unroll
  for (int hs = 0; hs < 10; ++hs) {
    if (hs <= 8) waitv<4>(); else waitv<2>();
    if (hs == 0) STAMP(2);
    BARRIER();
    if (hs == 2) STAMP(3); if (hs == 6) STAMP(4);
#pragma unroll
    for (int c = 0; c < 2; ++c) { trb[c] = (tr0 ^ ((2 * kh + c) << 6)) + 2 * (hs % 5) * SLOT; OPAQUE(trb[c]); }
    const unsigned xa = xb + 2 * (hs % 5) * SLOT;
    SCHED();
#pragma unroll
    for (int d = 0; d < 8; ++d) {
      if (hs > 0) qa[d & 1] = mfma32(af[(hs - 1) & 1][d], xf[(hs - 1) & 1][d >> 1], qa[d & 1]);
      SCHED();
      if (d < 4) xf[hs & 1][d] = ldsread16(xa ^ (32 * d));
      if (d < 4) af[hs & 1][d] = trpair(trb[d & 1] + 4096 * (d >> 1), trb[d & 1] + 4096 * (d >> 1) + 1024);
      else if (d < 6) {
        af[hs & 1][2 * d - 4] = trpair(trb[0] + 4096 * (d - 2), trb[0] + 4096 * (d - 2) + 1024);
        af[hs & 1][2 * d - 3] = trpair(trb[1] + 4096 * (d - 2), trb[1] + 4096 * (d - 2) + 1024);
      }
      if ((d & 1) == 1 && hs + 2 < 10) dmaU1(hs + 2, d >> 1);
      if (d < 2 && hs >= 8) dmaK1(hs - 8, d);
      SCHED();
    }
  }
#pragma unroll
  for (int d = 0; d < 8; ++d) qa[d & 1] = mfma32(af[1][d], xf[1][d >> 1], qa[d & 1]);
  dmaT(2);
  constexpr float C = 0.08838834764831845f * 1.4426950408889634f;
  qa[0] *= C; qa[1] *= C;
  bf16x8 qf[8];
  {
    const unsigned mine = lds0 + 6 * SLOT + wid * 4096 + lane * 16, other = lds0 + 6 * SLOT + (wid ^ 4) * 4096 + lane * 16;
    bf16x8 qm[4];
#pragma unroll
    for (int c = 0; c < 2; ++c)
#pragma unroll
      for (int s = 0; s < 2; ++s) {
        unsigned a0 = cvtpk(qa[c][8 * s], qa[c][8 * s + 1]), a1 = cvtpk(qa[c][8 * s + 2], qa[c][8 * s + 3]);
        unsigned b0 = cvtpk(qa[c][8 * s + 4], qa[c][8 * s + 5]), b1 = cvtpk(qa[c][8 * s + 6], qa[c][8 * s + 7]);
        auto s0 = __builtin_amdgcn_permlane32_swap(a0, b0, false, false);
        auto s1 = __builtin_amdgcn_permlane32_swap(a1, b1, false, false);
        u32x4 v = {s0[0], s1[0], s0[1], s1[1]};
        qm[2 * c + s] = __builtin_bit_cast(bf16x8, v);
        ldswrite16(mine + (2 * c + s) * 1024, qm[2 * c + s]);
      }
    BARRIER();
    dmaT(3);
#pragma unroll
    for (int i = 0; i < 4; ++i) {
      const bf16x8 o = ldsread16(other + i * 1024);
      qf[i] = kh ? o : qm[i];
      qf[4 + i] = kh ? qm[i] : o;
    }
  }
  STAMP(5);
  constexpr float THR = 8.0f;
  const int key = (q & 19) | ((q & 4) << 1) | ((q & 8) >> 1);
  const unsigned kr0 = lds0 + kh * 8192 + (unsigned)(key * 256 + ((hh ^ (key & 15)) << 4));
  unsigned krb[8];
  bf16x8 kf[2][8];
  f32x16 sc[2], scn, minit;
#pragma unroll
  for (int i = 0; i < 16; ++i) minit[i] = 0.f;
  bf16x8 pf[8][2];
  float alpha[8];
  unsigned resc = 0;
  float mref = 0.f, off = 0.f;
  bool offf = false;
  auto sm_max = [&](int t) {
    f32x16& v = sc[t & 1];
    float m0 = fmaxf(fmaxf(v[0], v[1]), v[2]), m1 = fmaxf(fmaxf(v[3], v[4]), v[5]);
    m0 = fmaxf(fmaxf(m0, v[6]), v[7]); m1 = fmaxf(fmaxf(m1, v[8]), v[9]);
    m0 = fmaxf(fmaxf(m0, v[10]), v[11]); m1 = fmaxf(fmaxf(m1, v[12]), v[13]);
    m0 = fmaxf(fmaxf(m0, v[14]), v[15]);
    m0 = fmaxf(m0, m1);
    auto sw = __builtin_amdgcn_permlane32_swap(__float_as_uint(m0), __float_as_uint(m0), false, false);
    m0 = fmaxf(__uint_as_float(sw[0]), __uint_as_float(sw[1]));
    alpha[t] = 1.f;
    m0 -= off;
    if (t <= 1 || __builtin_expect(offf || __any(m0 > THR), 0)) {
      const bool upd = t == 0 || __any(m0 > THR);
      const float dl = t == 0 ? m0 : (upd ? fmaxf(m0, 0.f) : 0.f);
      if (t > 0 && upd) { alpha[t] = __builtin_amdgcn_exp2f(-dl); resc |= 1u << t; }
      mref += dl;
      v -= off + dl;
      off = dl; offf = upd;
#pragma unroll
      for (int i = 0; i < 16; ++i) minit[i] = -mref;
    }
  };
  unsigned pw[8][8];
  float sh[8][8];
  auto sm_exp = [&](int t, int j) {
    float e[4];
#pragma unroll
    for (int i = 0; i < 4; ++i) e[i] = __builtin_amdgcn_exp2f(sc[t & 1][4 * j + i]);
    pw[t][2 * j] = cvtpk(e[0], e[1]);
    pw[t][2 * j + 1] = cvtpk(e[2], e[3]);
    asm volatile("" : "+v"(pw[t][2 * j]), "+v"(pw[t][2 * j + 1]));
  };
#pragma unroll
  for (int t = 0; t < 10; ++t) {
    if (t < 8) {
      waitv<6>();
      BARRIER();
      STAMP(6 + t);
      if ((t & 3) == 0) {
#pragma unroll
        for (int s = 0; s < 8; ++s) { krb[s] = (kr0 ^ (s << 5)) + t * SLOT; OPAQUE(krb[s]); }
      }
    }
    SCHED();
#pragma unroll
    for (int s = 0; s < 8; ++s) {
      if (t >= 1 && t <= 8) {
        if (s == 0) scn = mfma32(kf[(t - 1) & 1][0], qf[0], minit);
        else scn = mfma32(kf[(t - 1) & 1][s], qf[s], scn);
      }
      SCHED();
      if (t < 8 && s < 4) {
        kf[t & 1][2 * s] = ldsread16(krb[2 * s] + (t & 3) * SLOT);
        kf[t & 1][2 * s + 1] = ldsread16(krb[2 * s + 1] + (t & 3) * SLOT);
      }
      if (t >= 2 && s == 1) sm_max(t - 2);
      if (t >= 2 && (s == 3 || s == 5)) sm_exp(t - 2, (s - 3) >> 1);
      if (t >= 2 && s == 2) {
#pragma unroll
        for (int i = 0; i < 8; ++i) sh[t - 2][i] = sc[t & 1][8 + i];
      }
      SCHED();
    }
    if (t >= 1 && t <= 8) sc[(t - 1) & 1] = scn;
    if (t < 8) dmaT(4 + t);
  }
#pragma unroll
  for (int t = 0; t < 8; ++t) {
    u32x4 v = {pw[t][0], pw[t][1], pw[t][2], pw[t][3]};
    pf[t][0] = __builtin_bit_cast(bf16x8, v);
  }
  auto sm_exp2 = [&](int t, int j) {
    float e[4];
#pragma unroll
    for (int i = 0; i < 4; ++i) e[i] = __builtin_amdgcn_exp2f(sh[t][4 * j + i]);
    pw[t][4 + 2 * j] = cvtpk(e[0], e[1]);
    pw[t][5 + 2 * j] = cvtpk(e[2], e[3]);
    asm volatile("" : "+v"(pw[t][4 + 2 * j]), "+v"(pw[t][5 + 2 * j]));
    if (j == 1) {
      u32x4 v = {pw[t][4], pw[t][5], pw[t][6], pw[t][7]};
      pf[t][1] = __builtin_bit_cast(bf16x8, v);
    }
  };
  STAMP(14);
  f32x16 oa[4];
#pragma unroll
  for (int c = 0; c < 4; ++c)
#pragma unroll
    for (int i = 0; i < 16; ++i) oa[c][i] = 0.f;
  f32x16 la;
#pragma unroll
  for (int i = 0; i < 16; ++i) la[i] = 0.f;
  const bf16x8 ones = {0x3F80, 0x3F80, 0x3F80, 0x3F80, 0x3F80, 0x3F80, 0x3F80, 0x3F80};
  const int cs = 2 * (kh ^ 1);
#pragma unroll
  for (int t = 0; t < 8; ++t) {
    switch (t) {
      case 0: case 1: case 2: case 3: case 4: waitv<6>(); break;
      case 5: waitv<4>(); break;
      case 6: waitv<2>(); break;
      case 7: waitv<0>(); break;
    }
    BARRIER();
    STAMP(16 + t);
    if (t == 0 || t == 2 || t == 6) {
#pragma unroll
      for (int c = 0; c < 4; ++c) { trb[c] = (tr0 ^ ((c ^ cs) << 6)) + kh * 8192 + (t == 0 ? 8 * SLOT : t == 2 ? 0 : 4 * SLOT); OPAQUE(trb[c]); }
    }
    const unsigned so = (t < 2 ? t : t < 6 ? t - 2 : t - 6) * SLOT;
    if (t > 1) {
      if (__builtin_expect((resc >> (t - 1)) & 1, 0)) {
#pragma unroll
        for (int c = 0; c < 4; ++c) oa[c] *= alpha[t - 1];
        la *= alpha[t - 1];
      }
    }
    SCHED();
#pragma unroll
    for (int i = 0; i < 8; ++i) {
      if (t > 0) oa[i >> 1] = mfma32(af[(t - 1) & 1][i], pf[t - 1][i & 1], oa[i >> 1]);
      SCHED();
      if (i < 4) af[t & 1][i] = trpair(trb[i >> 1] + so + 4096 * (i & 1), trb[i >> 1] + so + 4096 * (i & 1) + 1024);
      else if (i < 6) {
        af[t & 1][2 * i - 4] = trpair(trb[i - 2] + so, trb[i - 2] + so + 1024);
        af[t & 1][2 * i - 3] = trpair(trb[i - 2] + so + 4096, trb[i - 2] + so + 4096 + 1024);
      }
      if (i == 2 || i == 6) sm_exp2(t, i >> 2);
      if (t == 5 && i == 4) { wv.own = wo_voff(wid, lane); OPAQUE(wv.own); }
      if (t == 6 && i == 0) { wv.p8 = wo_voff(8, lane); OPAQUE(wv.p8); }
      if (t == 6 && i == 4) { wv.p9 = wo_voff(9, lane); OPAQUE(wv.p9); }
      if (t == 7) dmaWo(wo, lds0, wid, wv, h, 0, i, i + 1);
      SCHED();
    }
    if (t > 0) { la = mfma32(ones, pf[t - 1][0], la); la = mfma32(ones, pf[t - 1][1], la); }
    SCHED();
    dmaT(12 + t);
  }
  if (__builtin_expect((resc >> 7) & 1, 0)) {
#pragma unroll
    for (int c = 0; c < 4; ++c) oa[c] *= alpha[7];
    la *= alpha[7];
  }
  la = mfma32(ones, pf[7][0], la); la = mfma32(ones, pf[7][1], la);
#pragma unroll
  for (int i = 0; i < 4; ++i) oa[i >> 1] = mfma32(af[1][i], pf[7][i & 1], oa[i >> 1]);
  STAMP(24);
  {
    const float l = la[0];
    const unsigned mine = lds0 + 6 * SLOT + wid * 4096 + lane * 16, other = lds0 + 6 * SLOT + (wid ^ 4) * 4096 + lane * 16;
    const unsigned mlm = lds0 + 61440 + wid * 256 + q * 8, mlo = lds0 + 61440 + (wid ^ 4) * 256 + q * 8;
    *(LDSP(f32x2))(size_t)mlm = f32x2{mref, l};
#pragma unroll
    for (int i = 0; i < 4; ++i) oa[2 + (i >> 1)] = mfma32(af[1][4 + i], pf[7][i & 1], oa[2 + (i >> 1)]);
#pragma unroll
    for (int c = 0; c < 2; ++c)
#pragma unroll
      for (int hf = 0; hf < 2; ++hf) {
        u32x4 v = {cvtpk(oa[c][8 * hf], oa[c][8 * hf + 1]), cvtpk(oa[c][8 * hf + 2], oa[c][8 * hf + 3]),
                   cvtpk(oa[c][8 * hf + 4], oa[c][8 * hf + 5]), cvtpk(oa[c][8 * hf + 6], oa[c][8 * hf + 7])};
        ldswrite16(mine + (2 * c + hf) * 1024, __builtin_bit_cast(bf16x8, v));
      }
    BARRIER();
    const f32x2 ml = *(LDSP(f32x2))(size_t)mlo;
    u32x4 pv[4];
#pragma unroll
    for (int i = 0; i < 4; ++i) pv[i] = __builtin_bit_cast(u32x4, ldsread16(other + i * 1024));
    const float m = fmaxf(mref, ml[0]);
    const float f0 = __builtin_amdgcn_exp2f(mref - m), f1 = __builtin_amdgcn_exp2f(ml[0] - m);
    const float linv = __builtin_amdgcn_rcpf(l * f0 + ml[1] * f1);
    const float s0 = f0 * linv, s1 = f1 * linv;
    uint16_t* op = ows + ((size_t)((b * 7 + 2 * qg + (q >> 4)) * 128 + h * 4 + 2 * kh) * 512) + (hh * 16 + (q & 15)) * 8;
    if (xrow < SQ) {
#pragma unroll
      for (int c = 0; c < 2; ++c)
#pragma unroll
        for (int hf = 0; hf < 2; ++hf) {
          u32x4 v;
#pragma unroll
          for (int i = 0; i < 4; ++i) {
            const unsigned w = pv[2 * c + hf][i];
            const float lo = fmaf(oa[2 + c][8 * hf + 2 * i], s0, __uint_as_float(w << 16) * s1);
            const float hi = fmaf(oa[2 + c][8 * hf + 2 * i + 1], s0, __uint_as_float(w & 0xffff0000u) * s1);
            v[i] = cvtpk(lo, hi);
          }
          asm volatile("global_store_dwordx4 %0, %1, off sc1\n\ts_nop 1" :: "v"(op + c * 512 + hf * 256), "v"(v) : "memory");
          ldswrite16(lds0 + P2_C + (2 * kh + c) * OSLOT + (2 * qg + (q >> 4)) * 1024 + ((2 * hf + hh) * 16 + (q & 15)) * 16, __builtin_bit_cast(bf16x8, v));
        }
    }
  }
  STAMP(25); RSTAMP(31);
  STAMP_FLUSH;
}

extern "C" __device__ unsigned long long amd_dispatch_id() __asm("llvm.amdgcn.dispatch.id");
typedef unsigned long long u64;
constexpr size_t WS_FO = (size_t)8 << 20;
constexpr size_t WS_FX = WS_FO + 65536;
constexpr size_t WS_X = (size_t)9 << 20;
template <int NM, int NN>
DI void oproj_body(const uint16_t* __restrict__ ows, const uint16_t* __restrict__ wo, uint16_t* __restrict__ out, char* __restrict__ ws,
                   unsigned lds0, int wid, int lane, int b, int h, int n0, const WoOff& wv, u64 tag) {
  constexpr int M0 = NM == 4 ? 0 : 4;
  const int r = lane & 15, g = lane >> 4, q4 = r >> 2, p = lane & 3;
  const int xg = h & 7, j = h >> 3;
  const unsigned o_voff = (unsigned)lane * 16;
  auto dmaO = [&](int i, unsigned dst) {
    const int head = xg + 8 * ((j + i) & 3);
#pragma unroll
    for (int k = 0; k < 4; ++k) {
      const int mt = wid < 7 ? wid : 6;
      dma1sc(lds0 + dst + k * OSLOT + mt * 1024, o_voff, ows + ((size_t)(b * 7 + mt) * 128 + head * 4 + k) * 512);
    }
  };
  const int trow = 16 * (g >> 1) + 4 * (g & 1) + q4;
  unsigned tra[NN];
#pragma unroll
  for (int c = 0; c < NN; ++c) tra[c] = lds0 + (unsigned)(trow * 320 + (((n0 + c) ^ (g & 1)) << 5) + 8 * p);
  const unsigned ofa = lds0 + M0 * 1024 + lane * 16;
  f32x4 acc[NN][NM];
#pragma unroll
  for (int c = 0; c < NN; ++c)
#pragma unroll
    for (int m = 0; m < NM; ++m) acc[c][m] = f32x4{0.f, 0.f, 0.f, 0.f};
  auto group = [&](int i, int obase) {
    bf16x8 af[2][NN], bf[2][NM];
#pragma unroll
    for (int c4 = 0; c4 < 5; ++c4) {
      if (c4 < 4) {
        const int wsl = wslot2(4 * i + c4);
#pragma unroll
        for (int c = 0; c < NN; ++c) {
          unsigned a = tra[c] + (wsl & ~0xffff);
          af[c4 & 1][c] = trpair(a + (wsl & 0xffff), a + (wsl & 0xffff) + 2560);
        }
        unsigned ob = ofa + obase;
#pragma unroll
        for (int m = 0; m < NM; ++m) bf[c4 & 1][m] = ldsread16(ob + c4 * OSLOT + m * 1024);
      }
      SCHED();
      if (c4 > 0) {
#pragma unroll
        for (int c = 0; c < NN; ++c)
#pragma unroll
          for (int m = 0; m < NM; ++m) acc[c][m] = mfma16(af[(c4 - 1) & 1][c], bf[(c4 - 1) & 1][m], acc[c][m]);
      }
      SCHED();
      if (i == 2) {
        if (c4 == 0) dmaO(3, P2_C);
        if (c4 == 1) dmaWo(wo, lds0, wid, wv, h, 2, 0, 3);
        if (c4 == 2) dmaWo(wo, lds0, wid, wv, h, 2, 3, 5);
      }
      SCHED();
    }
  };
  dmaWo(wo, lds0, wid, wv, h, 1);
  group(0, P2_C);
  if (wid == 0) {
    u64* f = (u64*)(ws + WS_FO) + b * 32 + xg + 8 * ((j + 1 + (lane % 3)) & 3);
    for (int spin = 0; spin < (1 << 22); ++spin) {
      if (__all(__hip_atomic_load(f, __ATOMIC_RELAXED, __HIP_MEMORY_SCOPE_AGENT) == tag)) break;
      __builtin_amdgcn_s_sleep(1);
    }
  }
  BARRIER();
  dmaO(1, P2_C); dmaO(2, 0);
  waitv<4>();
  BARRIER();
  group(1, P2_C);
  waitv<0>();
  BARRIER();
  group(2, 0);
  waitv<0>();
  BARRIER();
  group(3, P2_C);
  constexpr int NB = NM * NN, NW = (NB + 1) / 2;
  if (wid != xg) {
    const int dest = b * 32 + 8 * j + wid;
    char* xp = ws + WS_X + ((size_t)(dest * 8 + xg) * 6) * 1024;
#pragma unroll
    for (int i = 0; i < NW; ++i) {
      const f32x4 t0 = acc[(2 * i) / NM][(2 * i) % NM], t1 = 2 * i + 1 < NB ? acc[(2 * i + 1) / NM][(2 * i + 1) % NM] : t0;
      u32x4 v = {cvtpk(t0[0], t0[1]), cvtpk(t0[2], t0[3]), cvtpk(t1[0], t1[1]), cvtpk(t1[2], t1[3])};
      asm volatile("global_store_dwordx4 %0, %1, %2 offset:0 sc1\n\ts_nop 1" :: "v"(o_voff + (unsigned)(i * 1024)), "v"(v), "s"(xp) : "memory");
    }
    asm volatile("s_waitcnt vmcnt(0)" ::: "memory");
    if (lane == 0) __hip_atomic_store((u64*)(ws + WS_FX + (size_t)dest * 128) + xg, tag, __ATOMIC_RELAXED, __HIP_MEMORY_SCOPE_AGENT);
  } else {
#pragma unroll
    for (int k = 0; k < NB; ++k) ldswritef(lds0 + k * 1024 + lane * 16, acc[k / NM][k % NM]);
    u64* f = (u64*)(ws + WS_FX + (size_t)blockIdx.x * 128) + ((xg + 1 + (lane % 7)) & 7);
    for (int spin = 0; spin < (1 << 22); ++spin) {
      if (__all(__hip_atomic_load(f, __ATOMIC_RELAXED, __HIP_MEMORY_SCOPE_AGENT) == tag)) break;
      __builtin_amdgcn_s_sleep(1);
    }
  }
  BARRIER();
  const int xnm = xg < 4 ? 4 : 3, xnn = (xg < 2 || xg >= 6) ? 3 : 2, xnb = xnm * xnn;
  const int xm0 = xg < 4 ? 0 : 4, xn0 = xg < 2 ? 3 * xg : xg < 4 ? 2 * xg + 2 : xg < 6 ? 2 * (xg - 4) : 3 * xg - 14;
  if (2 * wid < xnb) {
    const char* xp = ws + WS_X + ((size_t)(blockIdx.x * 8) * 6 + wid) * 1024;
    u32x4 v[7];
#pragma unroll
    for (int s = 0; s < 7; ++s)
      asm volatile("global_load_dwordx4 %0, %1, %2 offset:0 sc1" : "=v"(v[s]) : "v"(o_voff), "s"(xp + (size_t)((xg + s + 1) & 7) * 6144) : "memory");
    f32x4 t[2];
    t[0] = ldsreadf(lds0 + (2 * wid) * 1024 + lane * 16);
    t[1] = ldsreadf(lds0 + (2 * wid + 1) * 1024 + lane * 16);
    asm volatile("s_waitcnt vmcnt(0)" : "+v"(v[0]), "+v"(v[1]), "+v"(v[2]), "+v"(v[3]), "+v"(v[4]), "+v"(v[5]), "+v"(v[6]) :: "memory");
#pragma unroll
    for (int e = 0; e < 2; ++e) {
#pragma unroll
      for (int s = 0; s < 7; ++s) {
        const unsigned w0 = v[s][2 * e], w1 = v[s][2 * e + 1];
        t[e] += f32x4{__uint_as_float(w0 << 16), __uint_as_float(w0 & 0xffff0000u), __uint_as_float(w1 << 16), __uint_as_float(w1 & 0xffff0000u)};
      }
      const int k = 2 * wid + e;
      if (k < xnb) {
        u32x2 o = {cvtpk(t[e][0], t[e][1]), cvtpk(t[e][2], t[e][3])};
        *(u32x2*)(out + (size_t)(b * SQ + (xm0 + k % xnm) * 16 + r) * DM + 160 * j + 16 * (xn0 + k / xnm) + 4 * g) = o;
      }
    }
  }
}
__global__ __launch_bounds__(512, 2) void fused_kernel(const uint16_t* __restrict__ x, const uint16_t* __restrict__ kc,
                                                       const uint16_t* __restrict__ vc, const uint16_t* __restrict__ wq,
                                                       const uint16_t* __restrict__ wo, uint16_t* __restrict__ out, char* __restrict__ ws) {
  extern __shared__ __attribute__((aligned(16))) char smem[];
  const int lane = threadIdx.x & 63;
  const int wid = __builtin_amdgcn_readfirstlane(threadIdx.x >> 6);
  const int h = blockIdx.x & 31, b = blockIdx.x >> 5;
  const unsigned lds0 = (unsigned)(size_t)smem;
  const u64 tag = (amd_dispatch_id() + 1) * 0x9E3779B97F4A7C15ull + (u64)(uintptr_t)__builtin_amdgcn_dispatch_ptr() * 0xC2B2AE3D27D4EB4Full + 0x165667B19E3779F9ull;
  WoOff wv;
  attn_body(x, kc, vc, wq, wo, (uint16_t*)ws, lds0, wid, lane, b, h, wv);
  asm volatile("s_waitcnt vmcnt(0)" ::: "memory");
  BARRIER();
  if (threadIdx.x == 0) __hip_atomic_store((u64*)(ws + WS_FO) + blockIdx.x, tag, __ATOMIC_RELAXED, __HIP_MEMORY_SCOPE_AGENT);
  const uint16_t* ows = (const uint16_t*)ws;
  if (wid < 2) oproj_body<4, 3>(ows, wo, out, ws, lds0, wid, lane, b, h, 3 * wid, wv, tag);
  else if (wid < 4) oproj_body<4, 2>(ows, wo, out, ws, lds0, wid, lane, b, h, 2 * wid + 2, wv, tag);
  else if (wid < 6) oproj_body<3, 2>(ows, wo, out, ws, lds0, wid, lane, b, h, 2 * (wid - 4), wv, tag);
  else oproj_body<3, 3>(ows, wo, out, ws, lds0, wid, lane, b, h, 3 * wid - 14, wv, tag);
}

extern "C" void kernel_launch(void* const* d_in, const int* in_sizes, int n_in, void* d_out, int out_size, void* d_ws,
                              size_t ws_size, hipStream_t stream) {
  const uint16_t* x = (const uint16_t*)d_in[0];
  const uint16_t* kc = (const uint16_t*)d_in[1];
  const uint16_t* vc = (const uint16_t*)d_in[2];
  const uint16_t* wq = (const uint16_t*)d_in[3];
  const uint16_t* wo = (const uint16_t*)d_in[4];
  (void)hipFuncSetAttribute((const void*)fused_kernel, hipFuncAttributeMaxDynamicSharedMemorySize, 10 * SLOT);
  fused_kernel<<<256, 512, 10 * SLOT, stream>>>(x, kc, vc, wq, wo, (uint16_t*)d_out, (char*)d_ws);
}
```

```cpp
#include <hip/hip_runtime.h>
#include <hip/hip_bf16.h>
#include <stdint.h>

typedef __attribute__((ext_vector_type(8))) short bf16x8;
typedef __attribute__((ext_vector_type(4))) short s16x4;
typedef __attribute__((ext_vector_type(4))) float f32x4;
typedef __attribute__((ext_vector_type(4))) unsigned u32x4;
typedef __attribute__((ext_vector_type(2))) unsigned u32x2;
#define LDSP(T) __attribute__((address_space(3))) T*
#define DI __device__ __forceinline__

constexpr int NB = 8, SQ = 112, DM = 640, NH = 32, SK = 512, DH = 128;
constexpr int SLOT = 16384;

typedef __attribute__((ext_vector_type(2))) float f32x2;
typedef __attribute__((ext_vector_type(2))) __bf16 bf16x2;
DI unsigned cvtpk(float lo, float hi) {
  f32x2 v = {lo, hi};
  return __builtin_bit_cast(unsigned, __builtin_convertvector(v, bf16x2));
}
DI f32x4 mfma16(bf16x8 a, bf16x8 b, f32x4 c) { return __builtin_amdgcn_mfma_f32_16x16x32_bf16(a, b, c, 0, 0, 0); }
DI s16x4 trread(unsigned addr) { return __builtin_amdgcn_ds_read_tr16_b64_v4i16((LDSP(s16x4))(size_t)addr); }
DI bf16x8 trpair(unsigned a0, unsigned a1) {
  s16x4 lo = trread(a0), hi = trread(a1);
  return __builtin_shufflevector(lo, hi, 0, 1, 2, 3, 4, 5, 6, 7);
}
DI bf16x8 ldsread16(unsigned addr) { return *(LDSP(bf16x8))(size_t)addr; }
DI void ldswrite16(unsigned addr, bf16x8 v) { *(LDSP(bf16x8))(size_t)addr = v; }

template <int OFF> DI void gload(bf16x8& dst, unsigned voff, const void* sbase) {
  asm volatile("global_load_dwordx4 %0, %1, %2 offset:%3" : "=v"(dst) : "v"(voff), "s"(sbase), "i"(OFF) : "memory");
}
DI void dma2(unsigned m0val, unsigned voff, const void* sbase) {
  unsigned keep;
  asm volatile("s_mov_b32 %0, m0\n\ts_mov_b32 m0, %1\n\ts_nop 0\n\t"
               "global_load_lds_dwordx4 %2, %3 nt\n\tglobal_load_lds_dwordx4 %2, %3 offset:1024 nt\n\t"
               "s_mov_b32 m0, %0"
               : "=&s"(keep) : "s"(m0val), "v"(voff), "s"(sbase) : "memory");
}
DI void dma1(unsigned m0val, unsigned voff, const void* sbase) {
  unsigned keep;
  asm volatile("s_mov_b32 %0, m0\n\ts_mov_b32 m0, %1\n\ts_nop 0\n\t"
               "global_load_lds_dwordx4 %2, %3\n\t"
               "s_mov_b32 m0, %0"
               : "=&s"(keep) : "s"(m0val), "v"(voff), "s"(sbase) : "memory");
}
#define STAMP_DECL
#define STAMP(i)
#define RSTAMP(i)
#define STAMP_FLUSH
#define BARRIER() asm volatile("s_waitcnt lgkmcnt(0)\n\ts_barrier" ::: "memory")
template <int N> DI void waitv() { asm volatile("s_waitcnt vmcnt(%0)" ::"i"(N) : "memory"); }
template <int N> DI void waitv2(bf16x8& a, bf16x8& b) { asm volatile("s_waitcnt vmcnt(%2)" : "+v"(a), "+v"(b) : "i"(N) : "memory"); }
template <int N> DI void waitv4(bf16x8& a, bf16x8& b, bf16x8& c, bf16x8& d) {
  asm volatile("s_waitcnt vmcnt(%4)" : "+v"(a), "+v"(b), "+v"(c), "+v"(d) : "i"(N) : "memory");
}

template <int N> DI void waitv6(bf16x8& a, bf16x8& b, bf16x8& c, bf16x8& d, bf16x8& e, bf16x8& f) {
  asm volatile("s_waitcnt vmcnt(%6)" : "+v"(a), "+v"(b), "+v"(c), "+v"(d), "+v"(e), "+v"(f) : "i"(N) : "memory");
}
template <int N> DI void waitv3(bf16x8& a, bf16x8& b, bf16x8& c) {
  asm volatile("s_waitcnt vmcnt(%3)" : "+v"(a), "+v"(b), "+v"(c) : "i"(N) : "memory");
}
DI void dma4(unsigned m0val, unsigned voff, const void* sbase) {
  unsigned keep;
  asm volatile("s_mov_b32 %0, m0\n\ts_mov_b32 m0, %1\n\ts_nop 0\n\t"
               "global_load_lds_dwordx4 %2, %3\n\tglobal_load_lds_dwordx4 %2, %3 offset:1024\n\t"
               "global_load_lds_dwordx4 %2, %3 offset:2048\n\tglobal_load_lds_dwordx4 %2, %3 offset:3072\n\t"
               "s_mov_b32 m0, %0"
               : "=&s"(keep) : "s"(m0val), "v"(voff), "s"(sbase) : "memory");
}
#define OPAQUE(x) asm volatile("" : "+v"(x))
#define SCHED() __builtin_amdgcn_sched_barrier(0)

typedef __attribute__((ext_vector_type(16))) float f32x16;
DI f32x16 mfma32(bf16x8 a, bf16x8 b, f32x16 c) { return __builtin_amdgcn_mfma_f32_32x32x16_bf16(a, b, c, 0, 0, 0); }
template <int OFF> DI void gload_nt(bf16x8& dst, unsigned voff, const void* sbase) {
  asm volatile("global_load_dwordx4 %0, %1, %2 offset:%3 nt" : "=v"(dst) : "v"(voff), "s"(sbase), "i"(OFF) : "memory");
}
DI void dma1nt(unsigned m0val, unsigned voff, const void* sbase) {
  unsigned keep;
  asm volatile("s_mov_b32 %0, m0\n\ts_mov_b32 m0, %1\n\ts_nop 0\n\t"
               "global_load_lds_dwordx4 %2, %3 nt\n\t"
               "s_mov_b32 m0, %0"
               : "=&s"(keep) : "s"(m0val), "v"(voff), "s"(sbase) : "memory");
}
DI void dma1sc(unsigned m0val, unsigned voff, const void* sbase) {
  unsigned keep;
  asm volatile("s_mov_b32 %0, m0\n\ts_mov_b32 m0, %1\n\ts_nop 0\n\t"
               "global_load_lds_dwordx4 %2, %3 sc1\n\t"
               "s_mov_b32 m0, %0"
               : "=&s"(keep) : "s"(m0val), "v"(voff), "s"(sbase) : "memory");
}
DI f32x4 ldsreadf(unsigned addr) { return *(LDSP(f32x4))(size_t)addr; }
DI void ldswritef(unsigned addr, f32x4 v) { *(LDSP(f32x4))(size_t)addr = v; }
DI f32x4 q4of(const f32x16& v, int i) { return f32x4{v[4 * i], v[4 * i + 1], v[4 * i + 2], v[4 * i + 3]}; }

__host__ __device__ constexpr int vslot(int t) { return t < 2 ? 8 + t : t - 2; }

DI void attn_body(const uint16_t* __restrict__ x, const uint16_t* __restrict__ kc, const uint16_t* __restrict__ vc,
                  const uint16_t* __restrict__ wq, uint16_t* __restrict__ ows, unsigned lds0, int wid, int lane, int b, int h, unsigned& t0, unsigned& t1) {
  const int r = lane & 15, g = lane >> 4;
  const int q = lane & 31, hh = lane >> 5;
  const int q4 = r >> 2, p = lane & 3, blk = g & 1;
  const int qg = wid & 3, kh = wid >> 2;
  STAMP_DECL RSTAMP(30); STAMP(0);
  const int rot = (b + 3 * (h >> 3)) % 10;
  const uint16_t* wq_s = wq + h * DH + (size_t)(8 * wid) * 4096;
  const uint16_t* x_s = x + (size_t)(b * SQ + 16 * (wid < 7 ? wid : 6)) * DM;
  const uint16_t* kb = kc + (size_t)(b * NH + h) * SK * DH + 8 * wid * DH;
  const uint16_t* vb = vc + (size_t)(b * NH + h) * SK * DH + 8 * wid * DH;
  auto unitof = [&](int hs) { int u = hs + rot; return u >= 10 ? u - 10 : u; };
  const unsigned w_voff = (unsigned)((g * 4096 + 8 * (r ^ (g << 2))) * 2);
  const unsigned v_voff = (unsigned)((g * DH + 8 * (r ^ (g << 2))) * 2);
  const unsigned k_voff0 = (unsigned)((g * DH + 8 * (r ^ (8 * (wid & 1) + g))) * 2);
  const unsigned k_voff1 = (unsigned)(((4 + g) * DH + 8 * (r ^ (8 * (wid & 1) + 4 + g))) * 2);
  const unsigned x_voff0 = (unsigned)(((lane >> 3) * DM + 8 * ((lane & 7) ^ (lane >> 4))) * 2);
  const unsigned x_voff1 = (unsigned)(((8 + (lane >> 3)) * DM + 8 * ((lane & 7) ^ (4 | (lane >> 4)))) * 2);
  auto dmaU1 = [&](int hs, int j) {
    const unsigned d = lds0 + 2 * (hs % 5) * SLOT + wid * 2048 + (j >> 1) * 1024;
    const int u = unitof(hs);
    if ((j & 1) == 0) dma1nt(d, w_voff, wq_s + (size_t)(64 * u + 4 * (j >> 1)) * 4096);
    else dma1(d + SLOT, (j >> 1) ? x_voff1 : x_voff0, x_s + 64 * u);
  };
  auto dmaU = [&](int hs) { dmaU1(hs, 0); dmaU1(hs, 1); dmaU1(hs, 2); dmaU1(hs, 3); };
  auto dmaK1 = [&](int t, int n) { dma1nt(lds0 + t * SLOT + wid * 2048 + n * 1024, n ? k_voff1 : k_voff0, kb + t * 64 * DH); };
  auto dmaV1 = [&](int t, int n) { dma1nt(lds0 + vslot(t) * SLOT + wid * 2048 + n * 1024, v_voff, vb + (t * 64 + 4 * n) * DH); };
  dmaU(0);
  asm volatile("s_barrier" ::: "memory");
  dmaU(1);
  STAMP(1);
  const unsigned tr0 = lds0 + (unsigned)((8 * hh + q4) * 256 + (q4 << 6) + 32 * blk + 8 * p);
  unsigned trb[4];
  const int xrow = 32 * qg + q;
  const unsigned xb = lds0 + SLOT + (unsigned)(xrow * 128 + ((hh ^ ((xrow >> 1) & 7)) << 4));

  f32x16 qa[2];
#pragma unroll
  for (int c = 0; c < 2; ++c)
#pragma unroll
    for (int i = 0; i < 16; ++i) qa[c][i] = 0.f;
  bf16x8 af[2][8], xf[2][4];
  auto dmaT = [&](int i) {
    if (i < 8) { dmaK1(i, 0); dmaK1(i, 1); } else if (i < 16) { dmaV1(i - 8, 0); dmaV1(i - 8, 1); }
  };
#pragma unroll
  for (int hs = 0; hs < 10; ++hs) {
    if (hs <= 8) waitv<4>(); else waitv<2>();
    if (hs == 0) STAMP(2);
    if (hs == 0) asm volatile("s_waitcnt lgkmcnt(0)" : "+s"(t0), "+s"(t1) :: "memory");
    BARRIER();
    if (hs == 2) STAMP(3); if (hs == 6) STAMP(4);
#pragma unroll
    for (int c = 0; c < 2; ++c) { trb[c] = (tr0 ^ ((2 * kh + c) << 6)) + 2 * (hs % 5) * SLOT; OPAQUE(trb[c]); }
    const unsigned xa = xb + 2 * (hs % 5) * SLOT;
    SCHED();
#pragma unroll
    for (int d = 0; d < 8; ++d) {
      if (hs > 0) qa[d & 1] = mfma32(af[(hs - 1) & 1][d], xf[(hs - 1) & 1][d >> 1], qa[d & 1]);
      SCHED();
      if (d < 4) xf[hs & 1][d] = ldsread16(xa ^ (32 * d));
      if (d < 4) af[hs & 1][d] = trpair(trb[d & 1] + 4096 * (d >> 1), trb[d & 1] + 4096 * (d >> 1) + 1024);
      else if (d < 6) {
        af[hs & 1][2 * d - 4] = trpair(trb[0] + 4096 * (d - 2), trb[0] + 4096 * (d - 2) + 1024);
        af[hs & 1][2 * d - 3] = trpair(trb[1] + 4096 * (d - 2), trb[1] + 4096 * (d - 2) + 1024);
      }
      if ((d & 1) == 1 && hs + 2 < 10) dmaU1(hs + 2, d >> 1);
      if (d < 2 && hs >= 8) dmaK1(hs - 8, d);
      SCHED();
    }
  }
#pragma unroll
  for (int d = 0; d < 8; ++d) qa[d & 1] = mfma32(af[1][d], xf[1][d >> 1], qa[d & 1]);
  dmaT(2);
  constexpr float C = 0.08838834764831845f * 1.4426950408889634f;
  qa[0] *= C; qa[1] *= C;
  bf16x8 qf[8];
  {
    const unsigned mine = lds0 + 6 * SLOT + wid * 4096 + lane * 16, other = lds0 + 6 * SLOT + (wid ^ 4) * 4096 + lane * 16;
    bf16x8 qm[4];
#pragma unroll
    for (int c = 0; c < 2; ++c)
#pragma unroll
      for (int s = 0; s < 2; ++s) {
        unsigned a0 = cvtpk(qa[c][8 * s], qa[c][8 * s + 1]), a1 = cvtpk(qa[c][8 * s + 2], qa[c][8 * s + 3]);
        unsigned b0 = cvtpk(qa[c][8 * s + 4], qa[c][8 * s + 5]), b1 = cvtpk(qa[c][8 * s + 6], qa[c][8 * s + 7]);
        auto s0 = __builtin_amdgcn_permlane32_swap(a0, b0, false, false);
        auto s1 = __builtin_amdgcn_permlane32_swap(a1, b1, false, false);
        u32x4 v = {s0[0], s1[0], s0[1], s1[1]};
        qm[2 * c + s] = __builtin_bit_cast(bf16x8, v);
        ldswrite16(mine + (2 * c + s) * 1024, qm[2 * c + s]);
      }
    BARRIER();
    dmaT(3);
#pragma unroll
    for (int i = 0; i < 4; ++i) {
      const bf16x8 o = ldsread16(other + i * 1024);
      qf[i] = kh ? o : qm[i];
      qf[4 + i] = kh ? qm[i] : o;
    }
  }
  STAMP(5);
  constexpr float THR = 8.0f;
  const int key = (q & 19) | ((q & 4) << 1) | ((q & 8) >> 1);
  const unsigned kr0 = lds0 + kh * 8192 + (unsigned)(key * 256 + ((hh ^ (key & 15)) << 4));
  unsigned krb[8];
  bf16x8 kf[2][8];
  f32x16 sc[2], scn, minit;
#pragma unroll
  for (int i = 0; i < 16; ++i) minit[i] = 0.f;
  bf16x8 pf[8][2];
  float alpha[8];
  unsigned resc = 0;
  float mref = 0.f, off = 0.f;
  bool offf = false;
  auto sm_max = [&](int t) {
    f32x16& v = sc[t & 1];
    float m0 = fmaxf(fmaxf(v[0], v[1]), v[2]), m1 = fmaxf(fmaxf(v[3], v[4]), v[5]);
    m0 = fmaxf(fmaxf(m0, v[6]), v[7]); m1 = fmaxf(fmaxf(m1, v[8]), v[9]);
    m0 = fmaxf(fmaxf(m0, v[10]), v[11]); m1 = fmaxf(fmaxf(m1, v[12]), v[13]);
    m0 = fmaxf(fmaxf(m0, v[14]), v[15]);
    m0 = fmaxf(m0, m1);
    auto sw = __builtin_amdgcn_permlane32_swap(__float_as_uint(m0), __float_as_uint(m0), false, false);
    m0 = fmaxf(__uint_as_float(sw[0]), __uint_as_float(sw[1]));
    alpha[t] = 1.f;
    m0 -= off;
    if (t <= 1 || __builtin_expect(offf || __any(m0 > THR), 0)) {
      const bool upd = t == 0 || __any(m0 > THR);
      const float dl = t == 0 ? m0 : (upd ? fmaxf(m0, 0.f) : 0.f);
      if (t > 0 && upd) { alpha[t] = __builtin_amdgcn_exp2f(-dl); resc |= 1u << t; }
      mref += dl;
      v -= off + dl;
      off = dl; offf = upd;
#pragma unroll
      for (int i = 0; i < 16; ++i) minit[i] = -mref;
    }
  };
  unsigned pw[8][8];
  float sh[8][8];
  auto sm_exp = [&](int t, int j) {
    float e[4];
#pragma unroll
    for (int i = 0; i < 4; ++i) e[i] = __builtin_amdgcn_exp2f(sc[t & 1][4 * j + i]);
    pw[t][2 * j] = cvtpk(e[0], e[1]);
    pw[t][2 * j + 1] = cvtpk(e[2], e[3]);
    asm volatile("" : "+v"(pw[t][2 * j]), "+v"(pw[t][2 * j + 1]));
  };
#pragma unroll
  for (int t = 0; t < 10; ++t) {
    if (t < 8) {
      waitv<6>();
      BARRIER();
      STAMP(6 + t);
      if ((t & 3) == 0) {
#pragma unroll
        for (int s = 0; s < 8; ++s) { krb[s] = (kr0 ^ (s << 5)) + t * SLOT; OPAQUE(krb[s]); }
      }
    }
    SCHED();
#pragma unroll
    for (int s = 0; s < 8; ++s) {
      if (t >= 1 && t <= 8) {
        if (s == 0) scn = mfma32(kf[(t - 1) & 1][0], qf[0], minit);
        else scn = mfma32(kf[(t - 1) & 1][s], qf[s], scn);
      }
      SCHED();
      if (t < 8 && s < 4) {
        kf[t & 1][2 * s] = ldsread16(krb[2 * s] + (t & 3) * SLOT);
        kf[t & 1][2 * s + 1] = ldsread16(krb[2 * s + 1] + (t & 3) * SLOT);
      }
      if (t >= 2 && s == 1) sm_max(t - 2);
      if (t >= 2 && (s == 3 || s == 5)) sm_exp(t - 2, (s - 3) >> 1);
      if (t >= 2 && s == 2) {
#pragma unroll
        for (int i = 0; i < 8; ++i) sh[t - 2][i] = sc[t & 1][8 + i];
      }
      SCHED();
    }
    if (t >= 1 && t <= 8) sc[(t - 1) & 1] = scn;
    if (t < 8) dmaT(4 + t);
  }
#pragma unroll
  for (int t = 0; t < 8; ++t) {
    u32x4 v = {pw[t][0], pw[t][1], pw[t][2], pw[t][3]};
    pf[t][0] = __builtin_bit_cast(bf16x8, v);
  }
  auto sm_exp2 = [&](int t, int j) {
    float e[4];
#pragma unroll
    for (int i = 0; i < 4; ++i) e[i] = __builtin_amdgcn_exp2f(sh[t][4 * j + i]);
    pw[t][4 + 2 * j] = cvtpk(e[0], e[1]);
    pw[t][5 + 2 * j] = cvtpk(e[2], e[3]);
    asm volatile("" : "+v"(pw[t][4 + 2 * j]), "+v"(pw[t][5 + 2 * j]));
    if (j == 1) {
      u32x4 v = {pw[t][4], pw[t][5], pw[t][6], pw[t][7]};
      pf[t][1] = __builtin_bit_cast(bf16x8, v);
    }
  };
  STAMP(14);
  f32x16 oa[4];
#pragma unroll
  for (int c = 0; c < 4; ++c)
#pragma unroll
    for (int i = 0; i < 16; ++i) oa[c][i] = 0.f;
  f32x16 la;
#pragma unroll
  for (int i = 0; i < 16; ++i) la[i] = 0.f;
  const bf16x8 ones = {0x3F80, 0x3F80, 0x3F80, 0x3F80, 0x3F80, 0x3F80, 0x3F80, 0x3F80};
  const int cs = 2 * (kh ^ 1);
#pragma unroll
  for (int t = 0; t < 8; ++t) {
    switch (t) {
      case 0: case 1: case 2: case 3: case 4: waitv<6>(); break;
      case 5: waitv<4>(); break;
      case 6: waitv<2>(); break;
      case 7: waitv<0>(); break;
    }
    BARRIER();
    STAMP(16 + t);
    if (t == 0 || t == 2 || t == 6) {
#pragma unroll
      for (int c = 0; c < 4; ++c) { trb[c] = (tr0 ^ ((c ^ cs) << 6)) + kh * 8192 + (t == 0 ? 8 * SLOT : t == 2 ? 0 : 4 * SLOT); OPAQUE(trb[c]); }
    }
    const unsigned so = (t < 2 ? t : t < 6 ? t - 2 : t - 6) * SLOT;
    if (t > 1) {
      if (__builtin_expect((resc >> (t - 1)) & 1, 0)) {
#pragma unroll
        for (int c = 0; c < 4; ++c) oa[c] *= alpha[t - 1];
        la *= alpha[t - 1];
      }
    }
    SCHED();
#pragma unroll
    for (int i = 0; i < 8; ++i) {
      if (t > 0) oa[i >> 1] = mfma32(af[(t - 1) & 1][i], pf[t - 1][i & 1], oa[i >> 1]);
      SCHED();
      if (i < 4) af[t & 1][i] = trpair(trb[i >> 1] + so + 4096 * (i & 1), trb[i >> 1] + so + 4096 * (i & 1) + 1024);
      else if (i < 6) {
        af[t & 1][2 * i - 4] = trpair(trb[i - 2] + so, trb[i - 2] + so + 1024);
        af[t & 1][2 * i - 3] = trpair(trb[i - 2] + so + 4096, trb[i - 2] + so + 4096 + 1024);
      }
      if (i == 2 || i == 6) sm_exp2(t, i >> 2);
      SCHED();
    }
    if (t > 0) { la = mfma32(ones, pf[t - 1][0], la); la = mfma32(ones, pf[t - 1][1], la); }
    SCHED();
    dmaT(12 + t);
  }
  if (__builtin_expect((resc >> 7) & 1, 0)) {
#pragma unroll
    for (int c = 0; c < 4; ++c) oa[c] *= alpha[7];
    la *= alpha[7];
  }
  la = mfma32(ones, pf[7][0], la); la = mfma32(ones, pf[7][1], la);
#pragma unroll
  for (int i = 0; i < 4; ++i) oa[i >> 1] = mfma32(af[1][i], pf[7][i & 1], oa[i >> 1]);
  STAMP(24);
  {
    const float l = la[0];
    const unsigned mine = lds0 + 6 * SLOT + wid * 4096 + lane * 16, other = lds0 + 6 * SLOT + (wid ^ 4) * 4096 + lane * 16;
    const unsigned mlm = lds0 + wid * 256 + q * 8, mlo = lds0 + (wid ^ 4) * 256 + q * 8;
    *(LDSP(f32x2))(size_t)mlm = f32x2{mref, l};
#pragma unroll
    for (int i = 0; i < 4; ++i) oa[2 + (i >> 1)] = mfma32(af[1][4 + i], pf[7][i & 1], oa[2 + (i >> 1)]);
#pragma unroll
    for (int c = 0; c < 2; ++c)
#pragma unroll
      for (int hf = 0; hf < 2; ++hf) {
        u32x4 v = {cvtpk(oa[c][8 * hf], oa[c][8 * hf + 1]), cvtpk(oa[c][8 * hf + 2], oa[c][8 * hf + 3]),
                   cvtpk(oa[c][8 * hf + 4], oa[c][8 * hf + 5]), cvtpk(oa[c][8 * hf + 6], oa[c][8 * hf + 7])};
        ldswrite16(mine + (2 * c + hf) * 1024, __builtin_bit_cast(bf16x8, v));
      }
    BARRIER();
    const f32x2 ml = *(LDSP(f32x2))(size_t)mlo;
    u32x4 pv[4];
#pragma unroll
    for (int i = 0; i < 4; ++i) pv[i] = __builtin_bit_cast(u32x4, ldsread16(other + i * 1024));
    const float m = fmaxf(mref, ml[0]);
    const float f0 = __builtin_amdgcn_exp2f(mref - m), f1 = __builtin_amdgcn_exp2f(ml[0] - m);
    const float linv = __builtin_amdgcn_rcpf(l * f0 + ml[1] * f1);
    const float s0 = f0 * linv, s1 = f1 * linv;
    uint16_t* op = ows + ((size_t)((b * 7 + 2 * qg + (q >> 4)) * 128 + h * 4 + 2 * kh) * 512) + (hh * 16 + (q & 15)) * 8;
    if (xrow < SQ) {
#pragma unroll
      for (int c = 0; c < 2; ++c)
#pragma unroll
        for (int hf = 0; hf < 2; ++hf) {
          u32x4 v;
#pragma unroll
          for (int i = 0; i < 4; ++i) {
            const unsigned w = pv[2 * c + hf][i];
            const float lo = fmaf(oa[2 + c][8 * hf + 2 * i], s0, __uint_as_float(w << 16) * s1);
            const float hi = fmaf(oa[2 + c][8 * hf + 2 * i + 1], s0, __uint_as_float(w & 0xffff0000u) * s1);
            v[i] = cvtpk(lo, hi);
          }
          asm volatile("global_store_dwordx4 %0, %1, off sc1\n\ts_nop 1" :: "v"(op + c * 512 + hf * 256), "v"(v) : "memory");
        }
    }
  }
  STAMP(25); RSTAMP(31);
  STAMP_FLUSH;
}

extern "C" __device__ unsigned long long amd_dispatch_id() __asm("llvm.amdgcn.dispatch.id");
typedef unsigned long long u64;
constexpr int KSLOT = 13312;
constexpr size_t WS_FO = (size_t)8 << 20;
constexpr size_t WS_FX = WS_FO + 65536;
constexpr size_t WS_X = (size_t)9 << 20;
template <bool LOADER>
DI void oproj_body(const uint16_t* __restrict__ ows, const uint16_t* __restrict__ wo, uint16_t* __restrict__ out, char* __restrict__ ws,
                   unsigned lds0, int wid, int lane, u64 tag) {
  const int xcd = blockIdx.x & 7, idx = blockIdx.x >> 3;
  const int kq = xcd >> 1, mh = xcd & 1, mgi = idx / 5, nb = idx % 5;
  const int mt0 = 28 * mh + (mgi < 4 ? 5 * mgi : 20 + 4 * (mgi - 4)), mcnt = mgi < 4 ? 5 : 4;
  const int r = lane & 15, g = lane >> 4, q4 = r >> 2, p = lane & 3;
  const int rot = (mgi + nb) & 7;
  const unsigned o_voff = (unsigned)lane * 16;
  if constexpr (LOADER) {
    const int lj = wid - 4;
    const unsigned w_voff0 = (unsigned)((g * DM + 8 * (r ^ (g << 1))) * 2), w_voff1 = (unsigned)((g * DM + 8 * (r ^ ((4 | g) << 1))) * 2);
    const uint16_t* wo_s = wo + (size_t)(1024 * kq + 32 * lj) * DM + 128 * nb;
    const uint16_t* o_s = ows + ((size_t)mt0 * 128 + 32 * kq + lj) * 512;
    auto issue = [&](int u, bool dw, bool dz) {
      const int uu = (u + rot) & 7;
      const unsigned d = lds0 + ((u % 3) * 4 + lj) * KSLOT;
#pragma unroll
      for (int i = 0; i < 8; ++i) {
        if (dw) dma1(d + i * 1024, (i & 1) ? w_voff1 : w_voff0, wo_s + (size_t)(128 * uu + 4 * i) * DM);
        if (dz && i < 5) dma1sc(d + 8192 + i * 1024, o_voff, o_s + ((size_t)(i < mcnt ? i : mcnt - 1) * 128 + 4 * uu) * 512);
      }
    };
    issue(0, true, false); issue(1, true, false); issue(2, true, false);
    BARRIER();
    issue(0, false, true); issue(1, false, true); issue(2, false, true);
#pragma unroll
    for (int u = 0; u < 8; ++u) {
      if (u == 0) waitv<10>(); else if (u == 1) waitv<5>(); else if (u < 7) waitv<13>(); else waitv<0>();
      BARRIER();
      if (u >= 1 && u + 2 < 8) issue(u + 2, true, true);
    }
    return;
  } else {
  const int np = wid;
  const int trow = 16 * (g >> 1) + 4 * (g & 1) + q4;
  const unsigned tra = lds0 + (unsigned)(trow * 256 + (((2 * np) ^ (trow & 7)) << 5) + 8 * p);
  const unsigned ofa = lds0 + 8192 + lane * 16;
  f32x4 acc[2][5];
#pragma unroll
  for (int c = 0; c < 2; ++c)
#pragma unroll
    for (int m = 0; m < 5; ++m) acc[c][m] = f32x4{0.f, 0.f, 0.f, 0.f};
  bf16x8 af[2][2], bf[2][5];
  if (wid == 0) {
    const int bb = (lane & 8) ? (mt0 + mcnt - 1) / 7 : mt0 / 7;
    u64* f = (u64*)(ws + WS_FO) + bb * 32 + 8 * kq + (lane & 7);
    for (int spin = 0; spin < (1 << 22); ++spin) {
      if (__all(__hip_atomic_load(f, __ATOMIC_RELAXED, __HIP_MEMORY_SCOPE_AGENT) == tag)) break;
      __builtin_amdgcn_s_sleep(1);
    }
  }
  BARRIER();
#pragma unroll
  for (int u = 0; u < 8; ++u) {
    BARRIER();
    const unsigned ub = (u % 3) * 4 * KSLOT;
    unsigned t0 = tra + ub, t1 = (tra ^ 32) + ub, ob = ofa + ub;
    OPAQUE(t0); OPAQUE(t1); OPAQUE(ob);
    SCHED();
#pragma unroll
    for (int j = 0; j < 4; ++j) {
#pragma unroll
      for (int m = 0; m < 5; ++m) {
        if (u > 0 || j > 0) {
          acc[0][m] = mfma16(af[(j + 1) & 1][0], bf[(j + 1) & 1][m], acc[0][m]);
          acc[1][m] = mfma16(af[(j + 1) & 1][1], bf[(j + 1) & 1][m], acc[1][m]);
        }
        SCHED();
        if (m == 0) af[j & 1][0] = trpair(t0 + j * KSLOT, t0 + j * KSLOT + 2048);
        if (m == 1) af[j & 1][1] = trpair(t1 + j * KSLOT, t1 + j * KSLOT + 2048);
        if (m < 2) bf[j & 1][m] = ldsread16(ob + j * KSLOT + m * 1024);
        if (m == 2) { bf[j & 1][2] = ldsread16(ob + j * KSLOT + 2048); bf[j & 1][3] = ldsread16(ob + j * KSLOT + 3072); bf[j & 1][4] = ldsread16(ob + j * KSLOT + 4096); }
        SCHED();
      }
    }
  }
#pragma unroll
  for (int m = 0; m < 5; ++m) {
    acc[0][m] = mfma16(af[1][0], bf[1][m], acc[0][m]);
    acc[1][m] = mfma16(af[1][1], bf[1][m], acc[1][m]);
  }
  const int dest = (idx << 3) | (np << 1) | mh;
  if (np != kq) {
    char* xp = ws + WS_X + ((size_t)(dest * 4 + kq) * 5) * 1024;
#pragma unroll
    for (int m = 0; m < 5; ++m) {
      u32x4 v = {cvtpk(acc[0][m][0], acc[0][m][1]), cvtpk(acc[0][m][2], acc[0][m][3]), cvtpk(acc[1][m][0], acc[1][m][1]), cvtpk(acc[1][m][2], acc[1][m][3])};
      asm volatile("global_store_dwordx4 %0, %1, %2 offset:0 sc1\n\ts_nop 1" :: "v"(o_voff + (unsigned)(m * 1024)), "v"(v), "s"(xp) : "memory");
    }
    asm volatile("s_waitcnt vmcnt(0)" ::: "memory");
    if (lane == 0) __hip_atomic_store((u64*)(ws + WS_FX + (size_t)dest * 128) + kq, tag, __ATOMIC_RELAXED, __HIP_MEMORY_SCOPE_AGENT);
    return;
  }
  {
    u64* f = (u64*)(ws + WS_FX + (size_t)blockIdx.x * 128) + ((kq + 1 + (lane % 3)) & 3);
    for (int spin = 0; spin < (1 << 22); ++spin) {
      if (__all(__hip_atomic_load(f, __ATOMIC_RELAXED, __HIP_MEMORY_SCOPE_AGENT) == tag)) break;
      __builtin_amdgcn_s_sleep(1);
    }
    asm volatile("s_barrier" ::: "memory");
    const char* xp = ws + WS_X + ((size_t)(blockIdx.x * 4) * 5) * 1024;
    u32x4 v[3][5];
#pragma unroll
    for (int s = 0; s < 3; ++s)
#pragma unroll
      for (int m = 0; m < 5; ++m)
        asm volatile("global_load_dwordx4 %0, %1, %2 offset:0 sc1" : "=v"(v[s][m]) : "v"(o_voff + (unsigned)(m * 1024)), "s"(xp + (size_t)((kq + s + 1) & 3) * 5120) : "memory");
    asm volatile("s_waitcnt vmcnt(0)" : "+v"(v[0][0]), "+v"(v[0][1]), "+v"(v[0][2]), "+v"(v[0][3]), "+v"(v[0][4]),
                 "+v"(v[1][0]), "+v"(v[1][1]), "+v"(v[1][2]), "+v"(v[1][3]), "+v"(v[1][4]),
                 "+v"(v[2][0]), "+v"(v[2][1]), "+v"(v[2][2]), "+v"(v[2][3]), "+v"(v[2][4]) :: "memory");
#pragma unroll
    for (int m = 0; m < 5; ++m)
#pragma unroll
      for (int c = 0; c < 2; ++c) {
        f32x4 t = acc[c][m];
#pragma unroll
        for (int s = 0; s < 3; ++s) {
          const unsigned w0 = v[s][m][2 * c], w1 = v[s][m][2 * c + 1];
          t += f32x4{__uint_as_float(w0 << 16), __uint_as_float(w0 & 0xffff0000u), __uint_as_float(w1 << 16), __uint_as_float(w1 & 0xffff0000u)};
        }
        if (m < mcnt) {
          u32x2 o = {cvtpk(t[0], t[1]), cvtpk(t[2], t[3])};
          *(u32x2*)(out + (size_t)((mt0 + m) * 16 + r) * DM + 128 * nb + 16 * (2 * np + c) + 4 * g) = o;
        }
      }
  }
  }
}
__global__ __launch_bounds__(512, 2) void fused_kernel(const uint16_t* __restrict__ x, const uint16_t* __restrict__ kc,
                                                       const uint16_t* __restrict__ vc, const uint16_t* __restrict__ wq,
                                                       const uint16_t* __restrict__ wo, uint16_t* __restrict__ out, char* __restrict__ ws) {
  extern __shared__ __attribute__((aligned(16))) char smem[];
  const int lane = threadIdx.x & 63;
  const int wid = __builtin_amdgcn_readfirstlane(threadIdx.x >> 6);
  const int h = blockIdx.x & 31, b = blockIdx.x >> 5;
  const unsigned lds0 = (unsigned)(size_t)smem;
  const u64 tag = (amd_dispatch_id() + 1) * 0x9E3779B97F4A7C15ull + (u64)(uintptr_t)__builtin_amdgcn_dispatch_ptr() * 0xC2B2AE3D27D4EB4Full + 0x165667B19E3779F9ull;
  unsigned t0, t1;
  {
    const int xcd = blockIdx.x & 7, idx = blockIdx.x >> 3, kq = xcd >> 1;
    const char* pa = wid == 0 ? (const char*)(kc + (size_t)blockIdx.x * SK * DH) : wid == 1 ? (const char*)(vc + (size_t)blockIdx.x * SK * DH)
                   : wid == 2 ? (const char*)(wo + (size_t)(1024 * kq) * DM) : wid == 3 ? (const char*)(wo + (size_t)(1024 * kq + 1023) * DM)
                   : wid == 4 ? ws + (size_t)b * 7 * 128 * 1024 : wid == 5 ? ws + (size_t)(28 * (xcd & 1) + 5 * (idx / 5)) * 128 * 1024
                   : wid == 6 ? ws + WS_X + (size_t)blockIdx.x * 20480 : (const char*)out;
    asm volatile("s_load_dword %0, %2, 0x0\n\ts_load_dword %1, %3, 0x0" : "=&s"(t0), "=&s"(t1) : "s"(pa), "s"(ws + WS_FO) : "memory");
  }
  attn_body(x, kc, vc, wq, (uint16_t*)ws, lds0, wid, lane, b, h, t0, t1);
  asm volatile("s_waitcnt vmcnt(0)" ::: "memory");
  BARRIER();
  if (threadIdx.x == 0) __hip_atomic_store((u64*)(ws + WS_FO) + blockIdx.x, tag, __ATOMIC_RELAXED, __HIP_MEMORY_SCOPE_AGENT);
  if (blockIdx.x >= 240) return;
  if (wid < 4) oproj_body<false>((const uint16_t*)ws, wo, out, ws, lds0, wid, lane, tag);
  else oproj_body<true>((const uint16_t*)ws, wo, out, ws, lds0, wid, lane, tag);
}

extern "C" void kernel_launch(void* const* d_in, const int* in_sizes, int n_in, void* d_out, int out_size, void* d_ws,
                              size_t ws_size, hipStream_t stream) {
  const uint16_t* x = (const uint16_t*)d_in[0];
  const uint16_t* kc = (const uint16_t*)d_in[1];
  const uint16_t* vc = (const uint16_t*)d_in[2];
  const uint16_t* wq = (const uint16_t*)d_in[3];
  const uint16_t* wo = (const uint16_t*)d_in[4];
  (void)hipFuncSetAttribute((const void*)fused_kernel, hipFuncAttributeMaxDynamicSharedMemorySize, 10 * SLOT);
  fused_kernel<<<256, 512, 10 * SLOT, stream>>>(x, kc, vc, wq, wo, (uint16_t*)d_out, (char*)d_ws);
}
```

```cpp
#include <hip/hip_runtime.h>
#include <hip/hip_bf16.h>
#include <stdint.h>

typedef __attribute__((ext_vector_type(8))) short bf16x8;
typedef __attribute__((ext_vector_type(4))) short s16x4;
typedef __attribute__((ext_vector_type(4))) float f32x4;
typedef __attribute__((ext_vector_type(4))) unsigned u32x4;
typedef __attribute__((ext_vector_type(2))) unsigned u32x2;
#define LDSP(T) __attribute__((address_space(3))) T*
#define DI __device__ __forceinline__

constexpr int NB = 8, SQ = 112, DM = 640, NH = 32, SK = 512, DH = 128;
constexpr int SLOT = 16384;

typedef __attribute__((ext_vector_type(2))) float f32x2;
typedef __attribute__((ext_vector_type(2))) __bf16 bf16x2;
DI unsigned cvtpk(float lo, float hi) {
  f32x2 v = {lo, hi};
  return __builtin_bit_cast(unsigned, __builtin_convertvector(v, bf16x2));
}
DI f32x4 mfma16(bf16x8 a, bf16x8 b, f32x4 c) { return __builtin_amdgcn_mfma_f32_16x16x32_bf16(a, b, c, 0, 0, 0); }
DI s16x4 trread(unsigned addr) { return __builtin_amdgcn_ds_read_tr16_b64_v4i16((LDSP(s16x4))(size_t)addr); }
DI bf16x8 trpair(unsigned a0, unsigned a1) {
  s16x4 lo = trread(a0), hi = trread(a1);
  return __builtin_shufflevector(lo, hi, 0, 1, 2, 3, 4, 5, 6, 7);
}
DI bf16x8 ldsread16(unsigned addr) { return *(LDSP(bf16x8))(size_t)addr; }
DI void ldswrite16(unsigned addr, bf16x8 v) { *(LDSP(bf16x8))(size_t)addr = v; }

DI void dma1(unsigned m0val, unsigned voff, const void* sbase) {
  unsigned keep;
  asm volatile("s_mov_b32 %0, m0\n\ts_mov_b32 m0, %1\n\ts_nop 0\n\t"
               "global_load_lds_dwordx4 %2, %3\n\t"
               "s_mov_b32 m0, %0"
               : "=&s"(keep) : "s"(m0val), "v"(voff), "s"(sbase) : "memory");
}
#define BARRIER() asm volatile("s_waitcnt lgkmcnt(0)\n\ts_barrier" ::: "memory")
template <int N> DI void waitv() { asm volatile("s_waitcnt vmcnt(%0)" ::"i"(N) : "memory"); }
#define OPAQUE(x) asm volatile("" : "+v"(x))
#define SCHED() __builtin_amdgcn_sched_barrier(0)

typedef __attribute__((ext_vector_type(16))) float f32x16;
DI f32x16 mfma32(bf16x8 a, bf16x8 b, f32x16 c) { return __builtin_amdgcn_mfma_f32_32x32x16_bf16(a, b, c, 0, 0, 0); }
DI void dma1nt(unsigned m0val, unsigned voff, const void* sbase) {
  unsigned keep;
  asm volatile("s_mov_b32 %0, m0\n\ts_mov_b32 m0, %1\n\ts_nop 0\n\t"
               "global_load_lds_dwordx4 %2, %3 nt\n\t"
               "s_mov_b32 m0, %0"
               : "=&s"(keep) : "s"(m0val), "v"(voff), "s"(sbase) : "memory");
}
DI void dma1sc(unsigned m0val, unsigned voff, const void* sbase) {
  unsigned keep;
  asm volatile("s_mov_b32 %0, m0\n\ts_mov_b32 m0, %1\n\ts_nop 0\n\t"
               "global_load_lds_dwordx4 %2, %3 sc1\n\t"
               "s_mov_b32 m0, %0"
               : "=&s"(keep) : "s"(m0val), "v"(voff), "s"(sbase) : "memory");
}
DI f32x4 ldsreadf(unsigned addr) { return *(LDSP(f32x4))(size_t)addr; }
DI void ldswritef(unsigned addr, f32x4 v) { *(LDSP(f32x4))(size_t)addr = v; }

__host__ __device__ constexpr int vslot(int t) { return t < 2 ? 8 + t : t - 2; }

DI void attn_body(const uint16_t* __restrict__ x, const uint16_t* __restrict__ kc, const uint16_t* __restrict__ vc,
                  const uint16_t* __restrict__ wq, uint16_t* __restrict__ ows, unsigned lds0, int wid, int lane, int b, int h, unsigned& t0, unsigned& t1) {
  OPAQUE(lane);
  const int r = lane & 15, g = lane >> 4;
  const int q = lane & 31, hh = lane >> 5;
  const int q4 = r >> 2, p = lane & 3, blk = g & 1;
  const int qg = wid & 3, kh = wid >> 2;
  const int rot = (b + 3 * (h >> 3)) % 10;
  const uint16_t* wq_s = wq + h * DH + (size_t)(8 * wid) * 4096;
  const uint16_t* x_s = x + (size_t)(b * SQ + 16 * (wid < 7 ? wid : 6)) * DM;
  const uint16_t* kb = kc + (size_t)(b * NH + h) * SK * DH + 8 * wid * DH;
  const uint16_t* vb = vc + (size_t)(b * NH + h) * SK * DH + 8 * wid * DH;
  auto unitof = [&](int hs) { int u = hs + rot; return u >= 10 ? u - 10 : u; };
  const unsigned w_voff = (unsigned)((g * 4096 + 8 * (r ^ (g << 2))) * 2);
  const unsigned v_voff = (unsigned)((g * DH + 8 * (r ^ (g << 2))) * 2);
  const unsigned k_voff0 = (unsigned)((g * DH + 8 * (r ^ (8 * (wid & 1) + g))) * 2);
  const unsigned k_voff1 = (unsigned)(((4 + g) * DH + 8 * (r ^ (8 * (wid & 1) + 4 + g))) * 2);
  const unsigned x_voff0 = (unsigned)(((lane >> 3) * DM + 8 * ((lane & 7) ^ (lane >> 4))) * 2);
  const unsigned x_voff1 = (unsigned)(((8 + (lane >> 3)) * DM + 8 * ((lane & 7) ^ (4 | (lane >> 4)))) * 2);
  auto dmaU1 = [&](int hs, int j) {
    const unsigned d = lds0 + 2 * (hs % 5) * SLOT + wid * 2048 + (j >> 1) * 1024;
    const int u = unitof(hs);
    if ((j & 1) == 0) dma1nt(d, w_voff, wq_s + (size_t)(64 * u + 4 * (j >> 1)) * 4096);
    else dma1(d + SLOT, (j >> 1) ? x_voff1 : x_voff0, x_s + 64 * u);
  };
  auto dmaU = [&](int hs) { dmaU1(hs, 0); dmaU1(hs, 1); dmaU1(hs, 2); dmaU1(hs, 3); };
  auto dmaK1 = [&](int t, int n) { dma1nt(lds0 + t * SLOT + wid * 2048 + n * 1024, n ? k_voff1 : k_voff0, kb + t * 64 * DH); };
  auto dmaV1 = [&](int t, int n) { dma1nt(lds0 + vslot(t) * SLOT + wid * 2048 + n * 1024, v_voff, vb + (t * 64 + 4 * n) * DH); };
  dmaU(0);
  asm volatile("s_barrier" ::: "memory");
  dmaU(1);
  const unsigned tr0 = lds0 + (unsigned)((8 * hh + q4) * 256 + (q4 << 6) + 32 * blk + 8 * p);
  unsigned trb[4];
  const int xrow = 32 * qg + q;
  const unsigned xb = lds0 + SLOT + (unsigned)(xrow * 128 + ((hh ^ ((xrow >> 1) & 7)) << 4));

  f32x16 qa[2];
#pragma unroll
  for (int c = 0; c < 2; ++c)
#pragma unroll
    for (int i = 0; i < 16; ++i) qa[c][i] = 0.f;
  bf16x8 af[2][8], xf[2][4];
  auto dmaT = [&](int i) {
    if (i < 8) { dmaK1(i, 0); dmaK1(i, 1); } else if (i < 16) { dmaV1(i - 8, 0); dmaV1(i - 8, 1); }
  };
#pragma unroll
  for (int hs = 0; hs < 10; ++hs) {
    if (hs <= 8) waitv<4>(); else waitv<2>();
    if (hs == 0) asm volatile("s_waitcnt lgkmcnt(0)" : "+s"(t0), "+s"(t1) :: "memory");
    BARRIER();
#pragma unroll
    for (int c = 0; c < 2; ++c) { trb[c] = (tr0 ^ ((2 * kh + c) << 6)) + 2 * (hs % 5) * SLOT; OPAQUE(trb[c]); }
    const unsigned xa = xb + 2 * (hs % 5) * SLOT;
    SCHED();
#pragma unroll
    for (int d = 0; d < 8; ++d) {
      if (hs > 0) qa[d & 1] = mfma32(af[(hs - 1) & 1][d], xf[(hs - 1) & 1][d >> 1], qa[d & 1]);
      SCHED();
      if (d < 4) xf[hs & 1][d] = ldsread16(xa ^ (32 * d));
      if (d < 4) af[hs & 1][d] = trpair(trb[d & 1] + 4096 * (d >> 1), trb[d & 1] + 4096 * (d >> 1) + 1024);
      else if (d < 6) {
        af[hs & 1][2 * d - 4] = trpair(trb[0] + 4096 * (d - 2), trb[0] + 4096 * (d - 2) + 1024);
        af[hs & 1][2 * d - 3] = trpair(trb[1] + 4096 * (d - 2), trb[1] + 4096 * (d - 2) + 1024);
      }
      if ((d & 1) == 1 && hs + 2 < 10) dmaU1(hs + 2, d >> 1);
      if (d < 2 && hs >= 8) dmaK1(hs - 8, d);
      SCHED();
    }
  }
#pragma unroll
  for (int d = 0; d < 8; ++d) qa[d & 1] = mfma32(af[1][d], xf[1][d >> 1], qa[d & 1]);
  dmaT(2);
  constexpr float C = 0.08838834764831845f * 1.4426950408889634f;
  bf16x8 qf[8];
  {
    const unsigned mine = lds0 + 6 * SLOT + wid * 4096 + lane * 16, other = lds0 + 6 * SLOT + (wid ^ 4) * 4096 + lane * 16;
    bf16x8 qm[4];
#pragma unroll
    for (int c = 0; c < 2; ++c)
#pragma unroll
      for (int s = 0; s < 2; ++s) {
        unsigned a0 = cvtpk(qa[c][8 * s], qa[c][8 * s + 1]), a1 = cvtpk(qa[c][8 * s + 2], qa[c][8 * s + 3]);
        unsigned b0 = cvtpk(qa[c][8 * s + 4], qa[c][8 * s + 5]), b1 = cvtpk(qa[c][8 * s + 6], qa[c][8 * s + 7]);
        auto s0 = __builtin_amdgcn_permlane32_swap(a0, b0, false, false);
        auto s1 = __builtin_amdgcn_permlane32_swap(a1, b1, false, false);
        u32x4 v = {s0[0], s1[0], s0[1], s1[1]};
        qm[2 * c + s] = __builtin_bit_cast(bf16x8, v);
        ldswrite16(mine + (2 * c + s) * 1024, qm[2 * c + s]);
      }
    BARRIER();
    dmaT(3);
#pragma unroll
    for (int i = 0; i < 4; ++i) {
      const bf16x8 o = ldsread16(other + i * 1024);
      qf[i] = kh ? o : qm[i];
      qf[4 + i] = kh ? qm[i] : o;
    }
  }
  constexpr float THR = 8.0f / C;
  const int key = (q & 19) | ((q & 4) << 1) | ((q & 8) >> 1);
  const unsigned kr0 = lds0 + kh * 8192 + (unsigned)(key * 256 + ((hh ^ (key & 15)) << 4));
  unsigned krb[8];
  bf16x8 kf[10];
  f32x16 sc[2], scn, minit;
#pragma unroll
  for (int i = 0; i < 16; ++i) minit[i] = 0.f;
  bf16x8 pf[8][2];
  float alpha[8];
  unsigned resc = 0;
  float mref = 0.f, off = 0.f, thr = THR;
  auto max3 = [](float a, float b, float c) { float d; asm("v_max3_f32 %0, %1, %2, %3" : "=v"(d) : "v"(a), "v"(b), "v"(c)); return d; };
  auto sm_max = [&](int t) {
    f32x16& v = sc[t & 1];
    float m0 = max3(v[0], v[1], v[2]), m1 = max3(v[3], v[4], v[5]);
    m0 = max3(m0, v[6], v[7]); m1 = max3(m1, v[8], v[9]);
    m0 = max3(m0, v[10], v[11]); m1 = max3(m1, v[12], v[13]);
    m0 = max3(m0, v[14], v[15]);
    m0 = max3(m0, m1, m1);
    alpha[t] = 1.f;
    if (t <= 1 || __builtin_expect(__any(m0 > thr), 0)) {
      auto sw = __builtin_amdgcn_permlane32_swap(__float_as_uint(m0), __float_as_uint(m0), false, false);
      m0 = fmaxf(__uint_as_float(sw[0]), __uint_as_float(sw[1])) - off;
      const bool upd = t == 0 || __any(m0 > THR);
      const float dl = t == 0 ? m0 : (upd ? fmaxf(m0, 0.f) : 0.f);
      if (t > 0 && upd) { alpha[t] = __builtin_amdgcn_exp2f(-dl * C); resc |= 1u << t; }
      mref += dl;
      v -= off + dl;
      off = dl; thr = upd ? -3.0e38f : THR;
#pragma unroll
      for (int i = 0; i < 16; ++i) minit[i] = -mref;
    }
  };
  unsigned pw[8][8];
  float sh[8][8];
  auto sm_exp = [&](int t, int j) {
    float e[4];
#pragma unroll
    for (int i = 0; i < 4; ++i) e[i] = __builtin_amdgcn_exp2f(sc[t & 1][4 * j + i] * C);
    pw[t][2 * j] = cvtpk(e[0], e[1]);
    pw[t][2 * j + 1] = cvtpk(e[2], e[3]);
    asm volatile("" : "+v"(pw[t][2 * j]), "+v"(pw[t][2 * j + 1]));
  };
#pragma unroll
  for (int t = 0; t < 10; ++t) {
    if (t < 8) {
      waitv<6>();
      BARRIER();
      if ((t & 3) == 0) {
#pragma unroll
        for (int s = 0; s < 8; ++s) { krb[s] = (kr0 ^ (s << 5)) + t * SLOT; OPAQUE(krb[s]); }
      }
    }
    SCHED();
#pragma unroll
    for (int s = 0; s < 8; ++s) {
      if (t >= 1 && t <= 8) {
        if (s == 0) scn = mfma32(kf[(8 * (t - 1)) % 10], qf[0], minit);
        else scn = mfma32(kf[(8 * (t - 1) + s) % 10], qf[s], scn);
      }
      SCHED();
      if (t < 8) {
        if (s == 0) { kf[(8 * t) % 10] = ldsread16(krb[0] + (t & 3) * SLOT); kf[(8 * t + 1) % 10] = ldsread16(krb[1] + (t & 3) * SLOT); }
        if (s < 6) kf[(8 * t + s + 2) % 10] = ldsread16(krb[s + 2] + (t & 3) * SLOT);
      }
      if (t >= 8 && s == 1) asm volatile("s_nop 15" : "+v"(sc[t & 1]));
      if (t >= 2 && s == 1) sm_max(t - 2);
      if (t >= 2 && (s == 3 || s == 5)) sm_exp(t - 2, (s - 3) >> 1);
      if (t >= 2 && s == 2) {
#pragma unroll
        for (int i = 0; i < 8; ++i) { sh[t - 2][i] = sc[t & 1][8 + i] * C; OPAQUE(sh[t - 2][i]); }
      }
      SCHED();
    }
    if (t >= 1 && t <= 8) sc[(t - 1) & 1] = scn;
    if (t < 8) dmaT(4 + t);
  }
#pragma unroll
  for (int t = 0; t < 8; ++t) {
    u32x4 v = {pw[t][0], pw[t][1], pw[t][2], pw[t][3]};
    pf[t][0] = __builtin_bit_cast(bf16x8, v);
  }
  auto sm_exp2 = [&](int t, int j) {
    float e[4];
#pragma unroll
    for (int i = 0; i < 4; ++i) e[i] = __builtin_amdgcn_exp2f(sh[t][4 * j + i]);
    pw[t][4 + 2 * j] = cvtpk(e[0], e[1]);
    pw[t][5 + 2 * j] = cvtpk(e[2], e[3]);
    asm volatile("" : "+v"(pw[t][4 + 2 * j]), "+v"(pw[t][5 + 2 * j]));
    if (j == 1) {
      u32x4 v = {pw[t][4], pw[t][5], pw[t][6], pw[t][7]};
      pf[t][1] = __builtin_bit_cast(bf16x8, v);
    }
  };
  f32x16 oa[4];
#pragma unroll
  for (int c = 0; c < 4; ++c)
#pragma unroll
    for (int i = 0; i < 16; ++i) oa[c][i] = 0.f;
  f32x16 la;
#pragma unroll
  for (int i = 0; i < 16; ++i) la[i] = 0.f;
  const bf16x8 ones = {0x3F80, 0x3F80, 0x3F80, 0x3F80, 0x3F80, 0x3F80, 0x3F80, 0x3F80};
  bf16x8 vf[10];
  const int cs = 2 * (kh ^ 1);
#pragma unroll
  for (int t = 0; t < 8; ++t) {
    switch (t) {
      case 0: case 1: case 2: case 3: case 4: waitv<6>(); break;
      case 5: waitv<4>(); break;
      case 6: waitv<2>(); break;
      case 7: waitv<0>(); break;
    }
    BARRIER();
    if (t == 0 || t == 2 || t == 6) {
#pragma unroll
      for (int c = 0; c < 4; ++c) { trb[c] = (tr0 ^ ((c ^ cs) << 6)) + kh * 8192 + (t == 0 ? 8 * SLOT : t == 2 ? 0 : 4 * SLOT); OPAQUE(trb[c]); }
    }
    const unsigned so = (t < 2 ? t : t < 6 ? t - 2 : t - 6) * SLOT;
    if (t > 1) {
      if (__builtin_expect((resc >> (t - 1)) & 1, 0)) {
        float a = alpha[t - 1];
        OPAQUE(a);
#pragma unroll
        for (int c = 0; c < 4; ++c) oa[c] *= a;
        la *= a;
      }
    }
    SCHED();
#pragma unroll
    for (int i = 0; i < 8; ++i) {
      if (t > 0) oa[i >> 1] = mfma32(vf[(8 * (t - 1) + i) % 10], pf[t - 1][i & 1], oa[i >> 1]);
      SCHED();
      if (i == 0) {
        vf[(8 * t) % 10] = trpair(trb[0] + so, trb[0] + so + 1024);
        vf[(8 * t + 1) % 10] = trpair(trb[0] + so + 4096, trb[0] + so + 4096 + 1024);
      }
      if (i < 6) vf[(8 * t + i + 2) % 10] = trpair(trb[(i + 2) >> 1] + so + 4096 * (i & 1), trb[(i + 2) >> 1] + so + 4096 * (i & 1) + 1024);
      if (i == 2 || i == 6) sm_exp2(t, i >> 2);
      SCHED();
    }
    if (t > 0) { la = mfma32(ones, pf[t - 1][0], la); la = mfma32(ones, pf[t - 1][1], la); }
    SCHED();
    dmaT(12 + t);
  }
  if (__builtin_expect((resc >> 7) & 1, 0)) {
    float a = alpha[7];
    OPAQUE(a);
#pragma unroll
    for (int c = 0; c < 4; ++c) oa[c] *= a;
    la *= a;
  }
  la = mfma32(ones, pf[7][0], la); la = mfma32(ones, pf[7][1], la);
#pragma unroll
  for (int i = 0; i < 4; ++i) oa[i >> 1] = mfma32(vf[(56 + i) % 10], pf[7][i & 1], oa[i >> 1]);
  {
    const float l = la[0];
    const unsigned mine = lds0 + 6 * SLOT + wid * 4096 + lane * 16, other = lds0 + 6 * SLOT + (wid ^ 4) * 4096 + lane * 16;
    const unsigned mlm = lds0 + wid * 256 + q * 8, mlo = lds0 + (wid ^ 4) * 256 + q * 8;
    *(LDSP(f32x2))(size_t)mlm = f32x2{mref, l};
#pragma unroll
    for (int i = 0; i < 4; ++i) oa[2 + (i >> 1)] = mfma32(vf[(60 + i) % 10], pf[7][i & 1], oa[2 + (i >> 1)]);
#pragma unroll
    for (int c = 0; c < 2; ++c)
#pragma unroll
      for (int hf = 0; hf < 2; ++hf) {
        u32x4 v = {cvtpk(oa[c][8 * hf], oa[c][8 * hf + 1]), cvtpk(oa[c][8 * hf + 2], oa[c][8 * hf + 3]),
                   cvtpk(oa[c][8 * hf + 4], oa[c][8 * hf + 5]), cvtpk(oa[c][8 * hf + 6], oa[c][8 * hf + 7])};
        ldswrite16(mine + (2 * c + hf) * 1024, __builtin_bit_cast(bf16x8, v));
      }
    BARRIER();
    const f32x2 ml = *(LDSP(f32x2))(size_t)mlo;
    u32x4 pv[4];
#pragma unroll
    for (int i = 0; i < 4; ++i) pv[i] = __builtin_bit_cast(u32x4, ldsread16(other + i * 1024));
    const float m = fmaxf(mref, ml[0]);
    const float f0 = __builtin_amdgcn_exp2f((mref - m) * C), f1 = __builtin_amdgcn_exp2f((ml[0] - m) * C);
    const float linv = __builtin_amdgcn_rcpf(l * f0 + ml[1] * f1);
    const float s0 = f0 * linv, s1 = f1 * linv;
    uint16_t* op = ows + ((size_t)((b * 7 + 2 * qg + (q >> 4)) * 128 + h * 4 + 2 * kh) * 512) + (hh * 16 + (q & 15)) * 8;
    if (xrow < SQ) {
#pragma unroll
      for (int c = 0; c < 2; ++c)
#pragma unroll
        for (int hf = 0; hf < 2; ++hf) {
          u32x4 v;
#pragma unroll
          for (int i = 0; i < 4; ++i) {
            const unsigned w = pv[2 * c + hf][i];
            const float lo = fmaf(oa[2 + c][8 * hf + 2 * i], s0, __uint_as_float(w << 16) * s1);
            const float hi = fmaf(oa[2 + c][8 * hf + 2 * i + 1], s0, __uint_as_float(w & 0xffff0000u) * s1);
            v[i] = cvtpk(lo, hi);
          }
          asm volatile("global_store_dwordx4 %0, %1, off sc1\n\ts_nop 1" :: "v"(op + c * 512 + hf * 256), "v"(v) : "memory");
        }
    }
  }
}

extern "C" __device__ unsigned long long amd_dispatch_id() __asm("llvm.amdgcn.dispatch.id");
typedef unsigned long long u64;
constexpr int KSLOT = 13312;
constexpr size_t WS_FO = (size_t)8 << 20;
constexpr size_t WS_FX = WS_FO + 65536;
constexpr size_t WS_X = (size_t)9 << 20;
template <bool LOADER>
DI void oproj_body(const uint16_t* __restrict__ ows, const uint16_t* __restrict__ wo, uint16_t* __restrict__ out, char* __restrict__ ws,
                   unsigned lds0, int wid, int lane, u64 tag) {
  OPAQUE(lane);
  const int xcd = blockIdx.x & 7, idx = blockIdx.x >> 3;
  const int kq = xcd >> 1, mh = xcd & 1, mgi = idx / 5, nb = idx % 5;
  const int mt0 = 28 * mh + (mgi < 4 ? 5 * mgi : 20 + 4 * (mgi - 4)), mcnt = mgi < 4 ? 5 : 4;
  const int r = lane & 15, g = lane >> 4, q4 = r >> 2, p = lane & 3;
  const int rot = (mgi + nb) & 7;
  const unsigned o_voff = (unsigned)lane * 16;
  if constexpr (LOADER) {
    const int lj = wid - 4;
    const unsigned w_voff0 = (unsigned)((g * DM + 8 * (r ^ (g << 1))) * 2), w_voff1 = (unsigned)((g * DM + 8 * (r ^ ((4 | g) << 1))) * 2);
    const uint16_t* wo_s = wo + (size_t)(1024 * kq + 32 * lj) * DM + 128 * nb;
    const uint16_t* o_s = ows + ((size_t)mt0 * 128 + 32 * kq + lj) * 512;
    auto issue = [&](int u, bool dw, bool dz) {
      const int uu = (u + rot) & 7;
      const unsigned d = lds0 + ((u % 3) * 4 + lj) * KSLOT;
#pragma unroll
      for (int i = 0; i < 8; ++i) {
        if (dw) dma1(d + i * 1024, (i & 1) ? w_voff1 : w_voff0, wo_s + (size_t)(128 * uu + 4 * i) * DM);
        if (dz && i < 5) dma1sc(d + 8192 + i * 1024, o_voff, o_s + ((size_t)(i < mcnt ? i : mcnt - 1) * 128 + 4 * uu) * 512);
      }
    };
    issue(0, true, false); issue(1, true, false); issue(2, true, false);
    BARRIER();
    issue(0, false, true); issue(1, false, true); issue(2, false, true);
#pragma unroll
    for (int u = 0; u < 8; ++u) {
      if (u == 0) waitv<10>(); else if (u == 1) waitv<5>(); else if (u < 7) waitv<13>(); else waitv<0>();
      BARRIER();
      if (u >= 1 && u + 2 < 8) issue(u + 2, true, true);
    }
    return;
  } else {
  const int np = wid;
  const int trow = 16 * (g >> 1) + 4 * (g & 1) + q4;
  const unsigned tra = lds0 + (unsigned)(trow * 256 + (((2 * np) ^ (trow & 7)) << 5) + 8 * p);
  const unsigned ofa = lds0 + 8192 + lane * 16;
  f32x4 acc[2][5];
#pragma unroll
  for (int c = 0; c < 2; ++c)
#pragma unroll
    for (int m = 0; m < 5; ++m) acc[c][m] = f32x4{0.f, 0.f, 0.f, 0.f};
  bf16x8 af[2][2], bf[2][5];
  if (wid == 0) {
    const int bb = (lane & 8) ? (mt0 + mcnt - 1) / 7 : mt0 / 7;
    u64* f = (u64*)(ws + WS_FO) + bb * 32 + 8 * kq + (lane & 7);
    for (int spin = 0; spin < (1 << 22); ++spin) {
      if (__all(__hip_atomic_load(f, __ATOMIC_RELAXED, __HIP_MEMORY_SCOPE_AGENT) == tag)) break;
      __builtin_amdgcn_s_sleep(1);
    }
  }
  BARRIER();
#pragma unroll
  for (int u = 0; u < 8; ++u) {
    BARRIER();
    const unsigned ub = (u % 3) * 4 * KSLOT;
    unsigned t0 = tra + ub, t1 = (tra ^ 32) + ub, ob = ofa + ub;
    OPAQUE(t0); OPAQUE(t1); OPAQUE(ob);
    SCHED();
#pragma unroll
    for (int j = 0; j < 4; ++j) {
#pragma unroll
      for (int m = 0; m < 5; ++m) {
        if (u > 0 || j > 0) {
          acc[0][m] = mfma16(af[(j + 1) & 1][0], bf[(j + 1) & 1][m], acc[0][m]);
          acc[1][m] = mfma16(af[(j + 1) & 1][1], bf[(j + 1) & 1][m], acc[1][m]);
        }
        SCHED();
        if (m == 0) af[j & 1][0] = trpair(t0 + j * KSLOT, t0 + j * KSLOT + 2048);
        if (m == 1) af[j & 1][1] = trpair(t1 + j * KSLOT, t1 + j * KSLOT + 2048);
        if (m < 2) bf[j & 1][m] = ldsread16(ob + j * KSLOT + m * 1024);
        if (m == 2) { bf[j & 1][2] = ldsread16(ob + j * KSLOT + 2048); bf[j & 1][3] = ldsread16(ob + j * KSLOT + 3072); bf[j & 1][4] = ldsread16(ob + j * KSLOT + 4096); }
        SCHED();
      }
    }
  }
#pragma unroll
  for (int m = 0; m < 5; ++m) {
    acc[0][m] = mfma16(af[1][0], bf[1][m], acc[0][m]);
    acc[1][m] = mfma16(af[1][1], bf[1][m], acc[1][m]);
  }
  const int dest = (idx << 3) | (np << 1) | mh;
  if (np != kq) {
    char* xp = ws + WS_X + ((size_t)(dest * 4 + kq) * 5) * 1024;
#pragma unroll
    for (int m = 0; m < 5; ++m) {
      u32x4 v = {cvtpk(acc[0][m][0], acc[0][m][1]), cvtpk(acc[0][m][2], acc[0][m][3]), cvtpk(acc[1][m][0], acc[1][m][1]), cvtpk(acc[1][m][2], acc[1][m][3])};
      asm volatile("global_store_dwordx4 %0, %1, %2 offset:0 sc1\n\ts_nop 1" :: "v"(o_voff + (unsigned)(m * 1024)), "v"(v), "s"(xp) : "memory");
    }
    asm volatile("s_waitcnt vmcnt(0)" ::: "memory");
    if (lane == 0) __hip_atomic_store((u64*)(ws + WS_FX + (size_t)dest * 128) + kq, tag, __ATOMIC_RELAXED, __HIP_MEMORY_SCOPE_AGENT);
    return;
  }
  {
    u64* f = (u64*)(ws + WS_FX + (size_t)blockIdx.x * 128) + ((kq + 1 + (lane % 3)) & 3);
    for (int spin = 0; spin < (1 << 22); ++spin) {
      if (__all(__hip_atomic_load(f, __ATOMIC_RELAXED, __HIP_MEMORY_SCOPE_AGENT) == tag)) break;
      __builtin_amdgcn_s_sleep(1);
    }
    asm volatile("s_barrier" ::: "memory");
    const char* xp = ws + WS_X + ((size_t)(blockIdx.x * 4) * 5) * 1024;
    u32x4 v[3][5];
#pragma unroll
    for (int s = 0; s < 3; ++s)
#pragma unroll
      for (int m = 0; m < 5; ++m)
        asm volatile("global_load_dwordx4 %0, %1, %2 offset:0 sc1" : "=v"(v[s][m]) : "v"(o_voff + (unsigned)(m * 1024)), "s"(xp + (size_t)((kq + s + 1) & 3) * 5120) : "memory");
    asm volatile("s_waitcnt vmcnt(0)" : "+v"(v[0][0]), "+v"(v[0][1]), "+v"(v[0][2]), "+v"(v[0][3]), "+v"(v[0][4]),
                 "+v"(v[1][0]), "+v"(v[1][1]), "+v"(v[1][2]), "+v"(v[1][3]), "+v"(v[1][4]),
                 "+v"(v[2][0]), "+v"(v[2][1]), "+v"(v[2][2]), "+v"(v[2][3]), "+v"(v[2][4]) :: "memory");
#pragma unroll
    for (int m = 0; m < 5; ++m)
#pragma unroll
      for (int c = 0; c < 2; ++c) {
        f32x4 t = acc[c][m];
#pragma unroll
        for (int s = 0; s < 3; ++s) {
          const unsigned w0 = v[s][m][2 * c], w1 = v[s][m][2 * c + 1];
          t += f32x4{__uint_as_float(w0 << 16), __uint_as_float(w0 & 0xffff0000u), __uint_as_float(w1 << 16), __uint_as_float(w1 & 0xffff0000u)};
        }
        if (m < mcnt) {
          u32x2 o = {cvtpk(t[0], t[1]), cvtpk(t[2], t[3])};
          *(u32x2*)(out + (size_t)((mt0 + m) * 16 + r) * DM + 128 * nb + 16 * (2 * np + c) + 4 * g) = o;
        }
      }
  }
  }
}
__global__ __launch_bounds__(512, 2) void fused_kernel(const uint16_t* __restrict__ x, const uint16_t* __restrict__ kc,
                                                       const uint16_t* __restrict__ vc, const uint16_t* __restrict__ wq,
                                                       const uint16_t* __restrict__ wo, uint16_t* __restrict__ out, char* __restrict__ ws) {
  extern __shared__ __attribute__((aligned(16))) char smem[];
  const int lane = threadIdx.x & 63;
  const int wid = __builtin_amdgcn_readfirstlane(threadIdx.x >> 6);
  const int h = blockIdx.x & 31, b = blockIdx.x >> 5;
  const unsigned lds0 = (unsigned)(size_t)smem;
  const u64 tag = (amd_dispatch_id() + 1) * 0x9E3779B97F4A7C15ull + (u64)(uintptr_t)__builtin_amdgcn_dispatch_ptr() * 0xC2B2AE3D27D4EB4Full + 0x165667B19E3779F9ull;
  unsigned t0, t1;
  {
    const int xcd = blockIdx.x & 7, idx = blockIdx.x >> 3, kq = xcd >> 1;
    const char* pa = wid == 0 ? (const char*)(kc + (size_t)blockIdx.x * SK * DH) : wid == 1 ? (const char*)(vc + (size_t)blockIdx.x * SK * DH)
                   : wid == 2 ? (const char*)(wo + (size_t)(1024 * kq) * DM) : wid == 3 ? (const char*)(wo + (size_t)(1024 * kq + 1023) * DM)
                   : wid == 4 ? ws + (size_t)b * 7 * 128 * 1024 : wid == 5 ? ws + (size_t)(28 * (xcd & 1) + 5 * (idx / 5)) * 128 * 1024
                   : wid == 6 ? ws + WS_X + (size_t)blockIdx.x * 20480 : (const char*)out;
    asm volatile("s_load_dword %0, %2, 0x0\n\ts_load_dword %1, %3, 0x0" : "=&s"(t0), "=&s"(t1) : "s"(pa), "s"(ws + WS_FO) : "memory");
  }
  attn_body(x, kc, vc, wq, (uint16_t*)ws, lds0, wid, lane, b, h, t0, t1);
  asm volatile("s_waitcnt vmcnt(0)" ::: "memory");
  BARRIER();
  if (threadIdx.x == 0) __hip_atomic_store((u64*)(ws + WS_FO) + blockIdx.x, tag, __ATOMIC_RELAXED, __HIP_MEMORY_SCOPE_AGENT);
  if (blockIdx.x >= 240) return;
  if (wid < 4) oproj_body<false>((const uint16_t*)ws, wo, out, ws, lds0, wid, lane, tag);
  else oproj_body<true>((const uint16_t*)ws, wo, out, ws, lds0, wid, lane, tag);
}

extern "C" void kernel_launch(void* const* d_in, const int* in_sizes, int n_in, void* d_out, int out_size, void* d_ws,
                              size_t ws_size, hipStream_t stream) {
  const uint16_t* x = (const uint16_t*)d_in[0];
  const uint16_t* kc = (const uint16_t*)d_in[1];
  const uint16_t* vc = (const uint16_t*)d_in[2];
  const uint16_t* wq = (const uint16_t*)d_in[3];
  const uint16_t* wo = (const uint16_t*)d_in[4];
  (void)hipFuncSetAttribute((const void*)fused_kernel, hipFuncAttributeMaxDynamicSharedMemorySize, 10 * SLOT);
  fused_kernel<<<256, 512, 10 * SLOT, stream>>>(x, kc, vc, wq, wo, (uint16_t*)d_out, (char*)d_ws);
}
```

```cpp
#include <hip/hip_runtime.h>
#include <hip/hip_bf16.h>
#include <stdint.h>

typedef __attribute__((ext_vector_type(8))) short bf16x8;
typedef __attribute__((ext_vector_type(4))) short s16x4;
typedef __attribute__((ext_vector_type(4))) float f32x4;
typedef __attribute__((ext_vector_type(4))) unsigned u32x4;
typedef __attribute__((ext_vector_type(2))) unsigned u32x2;
#define LDSP(T) __attribute__((address_space(3))) T*
#define DI __device__ __forceinline__

constexpr int NB = 8, SQ = 112, DM = 640, NH = 32, SK = 512, DH = 128;
constexpr int SLOT = 16384;

typedef __attribute__((ext_vector_type(2))) float f32x2;
typedef __attribute__((ext_vector_type(2))) __bf16 bf16x2;
DI unsigned cvtpk(float lo, float hi) {
  f32x2 v = {lo, hi};
  return __builtin_bit_cast(unsigned, __builtin_convertvector(v, bf16x2));
}
DI f32x4 mfma16(bf16x8 a, bf16x8 b, f32x4 c) { return __builtin_amdgcn_mfma_f32_16x16x32_bf16(a, b, c, 0, 0, 0); }
DI s16x4 trread(unsigned addr) { return __builtin_amdgcn_ds_read_tr16_b64_v4i16((LDSP(s16x4))(size_t)addr); }
DI bf16x8 trpair(unsigned a0, unsigned a1) {
  s16x4 lo = trread(a0), hi = trread(a1);
  return __builtin_shufflevector(lo, hi, 0, 1, 2, 3, 4, 5, 6, 7);
}
DI bf16x8 ldsread16(unsigned addr) { return *(LDSP(bf16x8))(size_t)addr; }
DI void ldswrite16(unsigned addr, bf16x8 v) { *(LDSP(bf16x8))(size_t)addr = v; }

DI void dma1(unsigned m0val, unsigned voff, const void* sbase) {
  unsigned keep;
  asm volatile("s_mov_b32 %0, m0\n\ts_mov_b32 m0, %1\n\ts_nop 0\n\t"
               "global_load_lds_dwordx4 %2, %3\n\t"
               "s_mov_b32 m0, %0"
               : "=&s"(keep) : "s"(m0val), "v"(voff), "s"(sbase) : "memory");
}
#define BARRIER() asm volatile("s_waitcnt lgkmcnt(0)\n\ts_barrier" ::: "memory")
template <int N> DI void waitv() { asm volatile("s_waitcnt vmcnt(%0)" ::"i"(N) : "memory"); }
#define OPAQUE(x) asm volatile("" : "+v"(x))
#define SCHED() __builtin_amdgcn_sched_barrier(0)

typedef __attribute__((ext_vector_type(16))) float f32x16;
DI f32x16 mfma32(bf16x8 a, bf16x8 b, f32x16 c) { return __builtin_amdgcn_mfma_f32_32x32x16_bf16(a, b, c, 0, 0, 0); }
DI void dma1nt(unsigned m0val, unsigned voff, const void* sbase) {
  unsigned keep;
  asm volatile("s_mov_b32 %0, m0\n\ts_mov_b32 m0, %1\n\ts_nop 0\n\t"
               "global_load_lds_dwordx4 %2, %3 nt\n\t"
               "s_mov_b32 m0, %0"
               : "=&s"(keep) : "s"(m0val), "v"(voff), "s"(sbase) : "memory");
}
DI void dma1sc(unsigned m0val, unsigned voff, const void* sbase) {
  unsigned keep;
  asm volatile("s_mov_b32 %0, m0\n\ts_mov_b32 m0, %1\n\ts_nop 0\n\t"
               "global_load_lds_dwordx4 %2, %3 sc1\n\t"
               "s_mov_b32 m0, %0"
               : "=&s"(keep) : "s"(m0val), "v"(voff), "s"(sbase) : "memory");
}
DI f32x4 ldsreadf(unsigned addr) { return *(LDSP(f32x4))(size_t)addr; }
DI void ldswritef(unsigned addr, f32x4 v) { *(LDSP(f32x4))(size_t)addr = v; }

__host__ __device__ constexpr int vslot(int t) { return t < 2 ? 8 + t : t - 2; }

DI void attn_body(const uint16_t* __restrict__ x, const uint16_t* __restrict__ kc, const uint16_t* __restrict__ vc,
                  const uint16_t* __restrict__ wq, uint16_t* __restrict__ ows, unsigned lds0, int wid, int lane, int b, int h, unsigned& t0, unsigned& t1) {
  OPAQUE(lane);
  const int r = lane & 15, g = lane >> 4;
  const int q = lane & 31, hh = lane >> 5;
  const int q4 = r >> 2, p = lane & 3, blk = g & 1;
  const int qg = wid & 3, kh = wid >> 2;
  const int rot = (b + 3 * (h >> 3)) % 10;
  const uint16_t* wq_s = wq + h * DH + (size_t)(8 * wid) * 4096;
  const uint16_t* x_s = x + (size_t)(b * SQ + 16 * (wid < 7 ? wid : 6)) * DM;
  const uint16_t* kb = kc + (size_t)(b * NH + h) * SK * DH + 8 * wid * DH;
  const uint16_t* vb = vc + (size_t)(b * NH + h) * SK * DH + 8 * wid * DH;
  auto unitof = [&](int hs) { int u = hs + rot; return u >= 10 ? u - 10 : u; };
  const unsigned w_voff = (unsigned)((g * 4096 + 8 * (r ^ (g << 2))) * 2);
  const unsigned v_voff = (unsigned)((g * DH + 8 * (r ^ (g << 2))) * 2);
  const unsigned k_voff0 = (unsigned)((g * DH + 8 * (r ^ (8 * (wid & 1) + g))) * 2);
  const unsigned k_voff1 = (unsigned)(((4 + g) * DH + 8 * (r ^ (8 * (wid & 1) + 4 + g))) * 2);
  const unsigned x_voff0 = (unsigned)(((lane >> 3) * DM + 8 * ((lane & 7) ^ (lane >> 4))) * 2);
  const unsigned x_voff1 = (unsigned)(((8 + (lane >> 3)) * DM + 8 * ((lane & 7) ^ (4 | (lane >> 4)))) * 2);
  auto dmaU1 = [&](int hs, int j) {
    const unsigned d = lds0 + 2 * (hs % 5) * SLOT + wid * 2048 + (j >> 1) * 1024;
    const int u = unitof(hs);
    if ((j & 1) == 0) dma1nt(d, w_voff, wq_s + (size_t)(64 * u + 4 * (j >> 1)) * 4096);
    else dma1(d + SLOT, (j >> 1) ? x_voff1 : x_voff0, x_s + 64 * u);
  };
  auto dmaU = [&](int hs) { dmaU1(hs, 0); dmaU1(hs, 1); dmaU1(hs, 2); dmaU1(hs, 3); };
  auto dmaK1 = [&](int t, int n) { dma1nt(lds0 + t * SLOT + wid * 2048 + n * 1024, n ? k_voff1 : k_voff0, kb + t * 64 * DH); };
  auto dmaV1 = [&](int t, int n) { dma1nt(lds0 + vslot(t) * SLOT + wid * 2048 + n * 1024, v_voff, vb + (t * 64 + 4 * n) * DH); };
  dmaU(0);
  asm volatile("s_barrier" ::: "memory");
  dmaU(1);
  const unsigned tr0 = lds0 + (unsigned)((8 * hh + q4) * 256 + (q4 << 6) + 32 * blk + 8 * p);
  unsigned trb[4];
  const int xrow = 32 * qg + q;
  const unsigned xb = lds0 + SLOT + (unsigned)(xrow * 128 + ((hh ^ ((xrow >> 1) & 7)) << 4));

  f32x16 qa[2];
#pragma unroll
  for (int c = 0; c < 2; ++c)
#pragma unroll
    for (int i = 0; i < 16; ++i) qa[c][i] = 0.f;
  bf16x8 af[2][8], xf[2][4];
  auto dmaT = [&](int i) {
    if (i < 8) { dmaK1(i, 0); dmaK1(i, 1); } else if (i < 16) { dmaV1(i - 8, 0); dmaV1(i - 8, 1); }
  };
#pragma unroll
  for (int hs = 0; hs < 10; ++hs) {
    if (hs <= 8) waitv<4>(); else waitv<2>();
    if (hs == 0) asm volatile("s_waitcnt lgkmcnt(0)" : "+s"(t0), "+s"(t1) :: "memory");
    BARRIER();
#pragma unroll
    for (int c = 0; c < 2; ++c) { trb[c] = (tr0 ^ ((2 * kh + c) << 6)) + 2 * (hs % 5) * SLOT; OPAQUE(trb[c]); }
    const unsigned xa = xb + 2 * (hs % 5) * SLOT;
    SCHED();
#pragma unroll
    for (int d = 0; d < 8; ++d) {
      if (hs > 0) qa[d & 1] = mfma32(af[(hs - 1) & 1][d], xf[(hs - 1) & 1][d >> 1], qa[d & 1]);
      SCHED();
      if (d < 4) xf[hs & 1][d] = ldsread16(xa ^ (32 * d));
      if (d < 4) af[hs & 1][d] = trpair(trb[d & 1] + 4096 * (d >> 1), trb[d & 1] + 4096 * (d >> 1) + 1024);
      else if (d < 6) {
        af[hs & 1][2 * d - 4] = trpair(trb[0] + 4096 * (d - 2), trb[0] + 4096 * (d - 2) + 1024);
        af[hs & 1][2 * d - 3] = trpair(trb[1] + 4096 * (d - 2), trb[1] + 4096 * (d - 2) + 1024);
      }
      if ((d & 1) == 1 && hs + 2 < 10) dmaU1(hs + 2, d >> 1);
      if (d < 2 && hs >= 8) dmaK1(hs - 8, d);
      SCHED();
    }
  }
#pragma unroll
  for (int d = 0; d < 8; ++d) qa[d & 1] = mfma32(af[1][d], xf[1][d >> 1], qa[d & 1]);
  dmaT(2);
  constexpr float C = 0.08838834764831845f * 1.4426950408889634f;
  bf16x8 qf[8];
  {
    const unsigned mine = lds0 + 6 * SLOT + wid * 4096 + lane * 16, other = lds0 + 6 * SLOT + (wid ^ 4) * 4096 + lane * 16;
    bf16x8 qm[4];
#pragma unroll
    for (int c = 0; c < 2; ++c)
#pragma unroll
      for (int s = 0; s < 2; ++s) {
        unsigned a0 = cvtpk(qa[c][8 * s], qa[c][8 * s + 1]), a1 = cvtpk(qa[c][8 * s + 2], qa[c][8 * s + 3]);
        unsigned b0 = cvtpk(qa[c][8 * s + 4], qa[c][8 * s + 5]), b1 = cvtpk(qa[c][8 * s + 6], qa[c][8 * s + 7]);
        auto s0 = __builtin_amdgcn_permlane32_swap(a0, b0, false, false);
        auto s1 = __builtin_amdgcn_permlane32_swap(a1, b1, false, false);
        u32x4 v = {s0[0], s1[0], s0[1], s1[1]};
        qm[2 * c + s] = __builtin_bit_cast(bf16x8, v);
        ldswrite16(mine + (2 * c + s) * 1024, qm[2 * c + s]);
      }
    BARRIER();
    dmaT(3);
#pragma unroll
    for (int i = 0; i < 4; ++i) {
      const bf16x8 o = ldsread16(other + i * 1024);
      qf[i] = kh ? o : qm[i];
      qf[4 + i] = kh ? qm[i] : o;
    }
  }
  constexpr float THR = 8.0f / C;
  const int key = (q & 19) | ((q & 4) << 1) | ((q & 8) >> 1);
  const unsigned kr0 = lds0 + kh * 8192 + (unsigned)(key * 256 + ((hh ^ (key & 15)) << 4));
  unsigned krb[8];
  bf16x8 kf[10];
  f32x16 sc[2], scn, minit;
#pragma unroll
  for (int i = 0; i < 16; ++i) minit[i] = 0.f;
  bf16x8 pf[8][2];
  float alpha[8];
  unsigned resc = 0;
  float mref = 0.f, off = 0.f, thr = THR;
  auto max3 = [](float a, float b, float c) { float d; asm("v_max3_f32 %0, %1, %2, %3" : "=v"(d) : "v"(a), "v"(b), "v"(c)); return d; };
  auto sm_max = [&](int t) {
    f32x16& v = sc[t & 1];
    float m0 = max3(v[0], v[1], v[2]), m1 = max3(v[3], v[4], v[5]);
    m0 = max3(m0, v[6], v[7]); m1 = max3(m1, v[8], v[9]);
    m0 = max3(m0, v[10], v[11]); m1 = max3(m1, v[12], v[13]);
    m0 = max3(m0, v[14], v[15]);
    m0 = max3(m0, m1, m1);
    alpha[t] = 1.f;
    if (t <= 1 || __builtin_expect(__any(m0 > thr), 0)) {
      auto sw = __builtin_amdgcn_permlane32_swap(__float_as_uint(m0), __float_as_uint(m0), false, false);
      m0 = fmaxf(__uint_as_float(sw[0]), __uint_as_float(sw[1])) - off;
      const bool upd = t == 0 || __any(m0 > THR);
      const float dl = t == 0 ? m0 : (upd ? fmaxf(m0, 0.f) : 0.f);
      if (t > 0 && upd) { alpha[t] = __builtin_amdgcn_exp2f(-dl * C); resc |= 1u << t; }
      mref += dl;
      v -= off + dl;
      off = dl; thr = upd ? -3.0e38f : THR;
#pragma unroll
      for (int i = 0; i < 16; ++i) minit[i] = -mref;
    }
  };
  unsigned pw[8][8];
  float sh[8][8];
  auto sm_exp = [&](int t, int j) {
    float e[4];
#pragma unroll
    for (int i = 0; i < 4; ++i) e[i] = __builtin_amdgcn_exp2f(sc[t & 1][4 * j + i] * C);
    pw[t][2 * j] = cvtpk(e[0], e[1]);
    pw[t][2 * j + 1] = cvtpk(e[2], e[3]);
    asm volatile("" : "+v"(pw[t][2 * j]), "+v"(pw[t][2 * j + 1]));
  };
#pragma unroll
  for (int t = 0; t < 10; ++t) {
    if (t < 8) {
      waitv<6>();
      BARRIER();
      if ((t & 3) == 0) {
#pragma unroll
        for (int s = 0; s < 8; ++s) { krb[s] = (kr0 ^ (s << 5)) + t * SLOT; OPAQUE(krb[s]); }
      }
    }
    SCHED();
#pragma unroll
    for (int s = 0; s < 8; ++s) {
      if (t >= 1 && t <= 8) {
        if (s == 0) scn = mfma32(kf[(8 * (t - 1)) % 10], qf[0], minit);
        else scn = mfma32(kf[(8 * (t - 1) + s) % 10], qf[s], scn);
      }
      SCHED();
      if (t < 8) {
        if (s == 0) { kf[(8 * t) % 10] = ldsread16(krb[0] + (t & 3) * SLOT); kf[(8 * t + 1) % 10] = ldsread16(krb[1] + (t & 3) * SLOT); }
        if (s < 6) kf[(8 * t + s + 2) % 10] = ldsread16(krb[s + 2] + (t & 3) * SLOT);
      }
      if (t >= 8 && s == 1) asm volatile("s_nop 15" : "+v"(sc[t & 1]));
      if (t >= 2 && s == 1) sm_max(t - 2);
      if (t >= 2 && (s == 3 || s == 5)) sm_exp(t - 2, (s - 3) >> 1);
      if (t >= 2 && s == 2) {
#pragma unroll
        for (int i = 0; i < 8; ++i) { sh[t - 2][i] = sc[t & 1][8 + i] * C; OPAQUE(sh[t - 2][i]); }
      }
      SCHED();
    }
    if (t >= 1 && t <= 8) sc[(t - 1) & 1] = scn;
    if (t < 8) dmaT(4 + t);
  }
#pragma unroll
  for (int t = 0; t < 8; ++t) {
    u32x4 v = {pw[t][0], pw[t][1], pw[t][2], pw[t][3]};
    pf[t][0] = __builtin_bit_cast(bf16x8, v);
  }
  auto sm_exp2 = [&](int t, int j) {
    float e[4];
#pragma unroll
    for (int i = 0; i < 4; ++i) e[i] = __builtin_amdgcn_exp2f(sh[t][4 * j + i]);
    pw[t][4 + 2 * j] = cvtpk(e[0], e[1]);
    pw[t][5 + 2 * j] = cvtpk(e[2], e[3]);
    asm volatile("" : "+v"(pw[t][4 + 2 * j]), "+v"(pw[t][5 + 2 * j]));
    if (j == 1) {
      u32x4 v = {pw[t][4], pw[t][5], pw[t][6], pw[t][7]};
      pf[t][1] = __builtin_bit_cast(bf16x8, v);
    }
  };
  f32x16 oa[4];
#pragma unroll
  for (int c = 0; c < 4; ++c)
#pragma unroll
    for (int i = 0; i < 16; ++i) oa[c][i] = 0.f;
  f32x16 la;
#pragma unroll
  for (int i = 0; i < 16; ++i) la[i] = 0.f;
  const bf16x8 ones = {0x3F80, 0x3F80, 0x3F80, 0x3F80, 0x3F80, 0x3F80, 0x3F80, 0x3F80};
  bf16x8 vf[10];
  const int cs = 2 * (kh ^ 1);
#pragma unroll
  for (int t = 0; t < 8; ++t) {
    switch (t) {
      case 0: case 1: case 2: case 3: case 4: waitv<6>(); break;
      case 5: waitv<4>(); break;
      case 6: waitv<2>(); break;
      case 7: waitv<0>(); break;
    }
    BARRIER();
    if (t == 0 || t == 2 || t == 6) {
#pragma unroll
      for (int c = 0; c < 4; ++c) { trb[c] = (tr0 ^ ((c ^ cs) << 6)) + kh * 8192 + (t == 0 ? 8 * SLOT : t == 2 ? 0 : 4 * SLOT); OPAQUE(trb[c]); }
    }
    const unsigned so = (t < 2 ? t : t < 6 ? t - 2 : t - 6) * SLOT;
    if (t > 1) {
      if (__builtin_expect((resc >> (t - 1)) & 1, 0)) {
        float a = alpha[t - 1];
        OPAQUE(a);
#pragma unroll
        for (int c = 0; c < 4; ++c) oa[c] *= a;
        la *= a;
      }
    }
    SCHED();
#pragma unroll
    for (int i = 0; i < 8; ++i) {
      if (t > 0) oa[i >> 1] = mfma32(vf[(8 * (t - 1) + i) % 10], pf[t - 1][i & 1], oa[i >> 1]);
      SCHED();
      if (i == 0) {
        vf[(8 * t) % 10] = trpair(trb[0] + so, trb[0] + so + 1024);
        vf[(8 * t + 1) % 10] = trpair(trb[0] + so + 4096, trb[0] + so + 4096 + 1024);
      }
      if (i < 6) vf[(8 * t + i + 2) % 10] = trpair(trb[(i + 2) >> 1] + so + 4096 * (i & 1), trb[(i + 2) >> 1] + so + 4096 * (i & 1) + 1024);
      if (i == 2 || i == 6) sm_exp2(t, i >> 2);
      SCHED();
    }
    if (t > 0) { la = mfma32(ones, pf[t - 1][0], la); la = mfma32(ones, pf[t - 1][1], la); }
    SCHED();
    dmaT(12 + t);
  }
  if (__builtin_expect((resc >> 7) & 1, 0)) {
    float a = alpha[7];
    OPAQUE(a);
#pragma unroll
    for (int c = 0; c < 4; ++c) oa[c] *= a;
    la *= a;
  }
  la = mfma32(ones, pf[7][0], la); la = mfma32(ones, pf[7][1], la);
#pragma unroll
  for (int i = 0; i < 4; ++i) oa[i >> 1] = mfma32(vf[(56 + i) % 10], pf[7][i & 1], oa[i >> 1]);
  {
    const float l = la[0];
    const unsigned mine = lds0 + 6 * SLOT + wid * 4096 + lane * 16, other = lds0 + 6 * SLOT + (wid ^ 4) * 4096 + lane * 16;
    const unsigned mlm = lds0 + wid * 256 + q * 8, mlo = lds0 + (wid ^ 4) * 256 + q * 8;
    *(LDSP(f32x2))(size_t)mlm = f32x2{mref, l};
#pragma unroll
    for (int i = 0; i < 4; ++i) oa[2 + (i >> 1)] = mfma32(vf[(60 + i) % 10], pf[7][i & 1], oa[2 + (i >> 1)]);
#pragma unroll
    for (int c = 0; c < 2; ++c)
#pragma unroll
      for (int hf = 0; hf < 2; ++hf) {
        u32x4 v = {cvtpk(oa[c][8 * hf], oa[c][8 * hf + 1]), cvtpk(oa[c][8 * hf + 2], oa[c][8 * hf + 3]),
                   cvtpk(oa[c][8 * hf + 4], oa[c][8 * hf + 5]), cvtpk(oa[c][8 * hf + 6], oa[c][8 * hf + 7])};
        ldswrite16(mine + (2 * c + hf) * 1024, __builtin_bit_cast(bf16x8, v));
      }
    BARRIER();
    const f32x2 ml = *(LDSP(f32x2))(size_t)mlo;
    u32x4 pv[4];
#pragma unroll
    for (int i = 0; i < 4; ++i) pv[i] = __builtin_bit_cast(u32x4, ldsread16(other + i * 1024));
    const float m = fmaxf(mref, ml[0]);
    const float f0 = __builtin_amdgcn_exp2f((mref - m) * C), f1 = __builtin_amdgcn_exp2f((ml[0] - m) * C);
    const float linv = __builtin_amdgcn_rcpf(l * f0 + ml[1] * f1);
    const float s0 = f0 * linv, s1 = f1 * linv;
    uint16_t* op = ows + ((size_t)((b * 7 + 2 * qg + (q >> 4)) * 128 + h * 4 + 2 * kh) * 512) + (hh * 16 + (q & 15)) * 8;
    if (xrow < SQ) {
#pragma unroll
      for (int c = 0; c < 2; ++c)
#pragma unroll
        for (int hf = 0; hf < 2; ++hf) {
          u32x4 v;
#pragma unroll
          for (int i = 0; i < 4; ++i) {
            const unsigned w = pv[2 * c + hf][i];
            const float lo = fmaf(oa[2 + c][8 * hf + 2 * i], s0, __uint_as_float(w << 16) * s1);
            const float hi = fmaf(oa[2 + c][8 * hf + 2 * i + 1], s0, __uint_as_float(w & 0xffff0000u) * s1);
            v[i] = cvtpk(lo, hi);
          }
          asm volatile("global_store_dwordx4 %0, %1, off sc1\n\ts_nop 1" :: "v"(op + c * 512 + hf * 256), "v"(v) : "memory");
        }
    }
  }
}

extern "C" __device__ unsigned long long amd_dispatch_id() __asm("llvm.amdgcn.dispatch.id");
typedef unsigned long long u64;
constexpr int KSLOT = 13312;
constexpr size_t WS_FO = (size_t)8 << 20;
constexpr size_t WS_FX = WS_FO + 65536;
constexpr size_t WS_X = (size_t)9 << 20;
template <bool LOADER>
DI void oproj_body(const uint16_t* __restrict__ ows, const uint16_t* __restrict__ wo, uint16_t* __restrict__ out, char* __restrict__ ws,
                   unsigned lds0, int wid, int lane, u64 tag) {
  OPAQUE(lane);
  const int xcd = blockIdx.x & 7, idx = blockIdx.x >> 3;
  const int kq = xcd >> 1, mh = xcd & 1, mgi = idx / 5, nb = idx % 5;
  const int mt0 = 28 * mh + (mgi < 4 ? 5 * mgi : 20 + 4 * (mgi - 4)), mcnt = mgi < 4 ? 5 : 4;
  const int r = lane & 15, g = lane >> 4, q4 = r >> 2, p = lane & 3;
  const int rot = (mgi + nb) & 7;
  const unsigned o_voff = (unsigned)lane * 16;
  if constexpr (LOADER) {
    const int lj = wid - 4;
    const unsigned w_voff0 = (unsigned)((g * DM + 8 * (r ^ (g << 1))) * 2), w_voff1 = (unsigned)((g * DM + 8 * (r ^ ((4 | g) << 1))) * 2);
    const uint16_t* wo_s = wo + (size_t)(1024 * kq + 32 * lj) * DM + 128 * nb;
    const uint16_t* o_s = ows + ((size_t)mt0 * 128 + 32 * kq + lj) * 512;
    auto issue = [&](int u, bool dw, bool dz) {
      const int uu = (u + rot) & 7;
      const unsigned d = lds0 + ((u % 3) * 4 + lj) * KSLOT;
#pragma unroll
      for (int i = 0; i < 8; ++i) {
        if (dw) dma1(d + i * 1024, (i & 1) ? w_voff1 : w_voff0, wo_s + (size_t)(128 * uu + 4 * i) * DM);
        if (dz && i < 5) dma1sc(d + 8192 + i * 1024, o_voff, o_s + ((size_t)(i < mcnt ? i : mcnt - 1) * 128 + 4 * uu) * 512);
      }
    };
    issue(0, true, false); issue(1, true, false); issue(2, true, false);
    BARRIER();
    issue(0, false, true); issue(1, false, true); issue(2, false, true);
#pragma unroll
    for (int u = 0; u < 8; ++u) {
      if (u == 0) waitv<10>(); else if (u == 1) waitv<5>(); else if (u < 7) waitv<13>(); else waitv<0>();
      BARRIER();
      if (u >= 1 && u + 2 < 8) issue(u + 2, true, true);
    }
    return;
  } else {
  const int np = wid;
  const int trow = 16 * (g >> 1) + 4 * (g & 1) + q4;
  const unsigned tra = lds0 + (unsigned)(trow * 256 + (((2 * np) ^ (trow & 7)) << 5) + 8 * p);
  const unsigned ofa = lds0 + 8192 + lane * 16;
  f32x4 acc[2][5];
#pragma unroll
  for (int c = 0; c < 2; ++c)
#pragma unroll
    for (int m = 0; m < 5; ++m) acc[c][m] = f32x4{0.f, 0.f, 0.f, 0.f};
  bf16x8 af[2][2], bf[2][5];
  if (wid == 0) {
    const int bb = (lane & 8) ? (mt0 + mcnt - 1) / 7 : mt0 / 7;
    u64* f = (u64*)(ws + WS_FO) + bb * 32 + 8 * kq + (lane & 7);
    for (int spin = 0; spin < (1 << 22); ++spin) {
      if (__all(__hip_atomic_load(f, __ATOMIC_RELAXED, __HIP_MEMORY_SCOPE_AGENT) == tag)) break;
      __builtin_amdgcn_s_sleep(1);
    }
  }
  BARRIER();
#pragma unroll
  for (int u = 0; u < 8; ++u) {
    BARRIER();
    const unsigned ub = (u % 3) * 4 * KSLOT;
    unsigned t0 = tra + ub, t1 = (tra ^ 32) + ub, ob = ofa + ub;
    OPAQUE(t0); OPAQUE(t1); OPAQUE(ob);
    SCHED();
#pragma unroll
    for (int j = 0; j < 4; ++j) {
#pragma unroll
      for (int m = 0; m < 5; ++m) {
        if (u > 0 || j > 0) {
          acc[0][m] = mfma16(af[(j + 1) & 1][0], bf[(j + 1) & 1][m], acc[0][m]);
          acc[1][m] = mfma16(af[(j + 1) & 1][1], bf[(j + 1) & 1][m], acc[1][m]);
        }
        SCHED();
        if (m == 0) af[j & 1][0] = trpair(t0 + j * KSLOT, t0 + j * KSLOT + 2048);
        if (m == 1) af[j & 1][1] = trpair(t1 + j * KSLOT, t1 + j * KSLOT + 2048);
        if (m < 2) bf[j & 1][m] = ldsread16(ob + j * KSLOT + m * 1024);
        if (m == 2) { bf[j & 1][2] = ldsread16(ob + j * KSLOT + 2048); bf[j & 1][3] = ldsread16(ob + j * KSLOT + 3072); bf[j & 1][4] = ldsread16(ob + j * KSLOT + 4096); }
        SCHED();
      }
    }
  }
#pragma unroll
  for (int m = 0; m < 5; ++m) {
    acc[0][m] = mfma16(af[1][0], bf[1][m], acc[0][m]);
    acc[1][m] = mfma16(af[1][1], bf[1][m], acc[1][m]);
  }
  const int dest = (idx << 3) | (np << 1) | mh;
  if (np != kq) {
    char* xp = ws + WS_X + ((size_t)(dest * 4 + kq) * 5) * 1024;
#pragma unroll
    for (int m = 0; m < 5; ++m) {
      u32x4 v = {cvtpk(acc[0][m][0], acc[0][m][1]), cvtpk(acc[0][m][2], acc[0][m][3]), cvtpk(acc[1][m][0], acc[1][m][1]), cvtpk(acc[1][m][2], acc[1][m][3])};
      asm volatile("global_store_dwordx4 %0, %1, %2 offset:0 sc1\n\ts_nop 1" :: "v"(o_voff + (unsigned)(m * 1024)), "v"(v), "s"(xp) : "memory");
    }
    asm volatile("s_waitcnt vmcnt(0)" ::: "memory");
    if (lane == 0) __hip_atomic_store((u64*)(ws + WS_FX + (size_t)dest * 128) + kq, tag, __ATOMIC_RELAXED, __HIP_MEMORY_SCOPE_AGENT);
    return;
  }
  {
    u64* f = (u64*)(ws + WS_FX + (size_t)blockIdx.x * 128) + ((kq + 1 + (lane % 3)) & 3);
    for (int spin = 0; spin < (1 << 22); ++spin) {
      if (__all(__hip_atomic_load(f, __ATOMIC_RELAXED, __HIP_MEMORY_SCOPE_AGENT) == tag)) break;
      __builtin_amdgcn_s_sleep(1);
    }
    asm volatile("s_barrier" ::: "memory");
    const char* xp = ws + WS_X + ((size_t)(blockIdx.x * 4) * 5) * 1024;
    u32x4 v[3][5];
#pragma unroll
    for (int s = 0; s < 3; ++s)
#pragma unroll
      for (int m = 0; m < 5; ++m)
        asm volatile("global_load_dwordx4 %0, %1, %2 offset:0 sc1" : "=v"(v[s][m]) : "v"(o_voff + (unsigned)(m * 1024)), "s"(xp + (size_t)((kq + s + 1) & 3) * 5120) : "memory");
    asm volatile("s_waitcnt vmcnt(0)" : "+v"(v[0][0]), "+v"(v[0][1]), "+v"(v[0][2]), "+v"(v[0][3]), "+v"(v[0][4]),
                 "+v"(v[1][0]), "+v"(v[1][1]), "+v"(v[1][2]), "+v"(v[1][3]), "+v"(v[1][4]),
                 "+v"(v[2][0]), "+v"(v[2][1]), "+v"(v[2][2]), "+v"(v[2][3]), "+v"(v[2][4]) :: "memory");
#pragma unroll
    for (int m = 0; m < 5; ++m)
#pragma unroll
      for (int c = 0; c < 2; ++c) {
        f32x4 t = acc[c][m];
#pragma unroll
        for (int s = 0; s < 3; ++s) {
          const unsigned w0 = v[s][m][2 * c], w1 = v[s][m][2 * c + 1];
          t += f32x4{__uint_as_float(w0 << 16), __uint_as_float(w0 & 0xffff0000u), __uint_as_float(w1 << 16), __uint_as_float(w1 & 0xffff0000u)};
        }
        if (m < mcnt) {
          u32x2 o = {cvtpk(t[0], t[1]), cvtpk(t[2], t[3])};
          *(u32x2*)(out + (size_t)((mt0 + m) * 16 + r) * DM + 128 * nb + 16 * (2 * np + c) + 4 * g) = o;
        }
      }
  }
  }
}
__global__ __launch_bounds__(512, 2) void fused_kernel(const uint16_t* __restrict__ x, const uint16_t* __restrict__ kc,
                                                       const uint16_t* __restrict__ vc, const uint16_t* __restrict__ wq,
                                                       const uint16_t* __restrict__ wo, uint16_t* __restrict__ out, char* __restrict__ ws) {
  extern __shared__ __attribute__((aligned(16))) char smem[];
  const int lane = threadIdx.x & 63;
  const int wid = __builtin_amdgcn_readfirstlane(threadIdx.x >> 6);
  const int h = blockIdx.x & 31, b = blockIdx.x >> 5;
  const unsigned lds0 = (unsigned)(size_t)smem;
  const u64 tag = (amd_dispatch_id() + 1) * 0x9E3779B97F4A7C15ull + (u64)(uintptr_t)__builtin_amdgcn_dispatch_ptr() * 0xC2B2AE3D27D4EB4Full + 0x165667B19E3779F9ull;
  unsigned t0, t1;
  {
    const int wi = (blockIdx.x >> 3) * 8 + wid;
    const char* pa = wi < 80 ? (const char*)wq + (size_t)wi * 65536 : wi < 160 ? (const char*)wo + (size_t)(wi - 80) * 65536
                   : wi < 177 ? (const char*)x + (size_t)(wi - 160) * 65536 : wi < 194 ? (const char*)out + (size_t)(wi - 177) * 65536
                   : ws + WS_FO + (size_t)(wi - 194) * 98304;
    const char* pb = wid < 2 ? (const char*)(kc + (size_t)blockIdx.x * SK * DH) + wid * 65536
                   : wid < 4 ? (const char*)(vc + (size_t)blockIdx.x * SK * DH) + (wid - 2) * 65536
                   : ws + (size_t)((blockIdx.x >> 3) * 4 + wid - 4) * 57344;
    asm volatile("s_load_dword %0, %2, 0x0\n\ts_load_dword %1, %3, 0x0" : "=&s"(t0), "=&s"(t1) : "s"(pa), "s"(pb) : "memory");
  }
  attn_body(x, kc, vc, wq, (uint16_t*)ws, lds0, wid, lane, b, h, t0, t1);
  asm volatile("s_waitcnt vmcnt(0)" ::: "memory");
  BARRIER();
  if (threadIdx.x == 0) __hip_atomic_store((u64*)(ws + WS_FO) + blockIdx.x, tag, __ATOMIC_RELAXED, __HIP_MEMORY_SCOPE_AGENT);
  if (blockIdx.x >= 240) return;
  if (wid < 4) oproj_body<false>((const uint16_t*)ws, wo, out, ws, lds0, wid, lane, tag);
  else oproj_body<true>((const uint16_t*)ws, wo, out, ws, lds0, wid, lane, tag);
}

extern "C" void kernel_launch(void* const* d_in, const int* in_sizes, int n_in, void* d_out, int out_size, void* d_ws,
                              size_t ws_size, hipStream_t stream) {
  const uint16_t* x = (const uint16_t*)d_in[0];
  const uint16_t* kc = (const uint16_t*)d_in[1];
  const uint16_t* vc = (const uint16_t*)d_in[2];
  const uint16_t* wq = (const uint16_t*)d_in[3];
  const uint16_t* wo = (const uint16_t*)d_in[4];
  (void)hipFuncSetAttribute((const void*)fused_kernel, hipFuncAttributeMaxDynamicSharedMemorySize, 10 * SLOT);
  fused_kernel<<<256, 512, 10 * SLOT, stream>>>(x, kc, vc, wq, wo, (uint16_t*)d_out, (char*)d_ws);
}
```

```cpp
#include <hip/hip_runtime.h>
#include <hip/hip_bf16.h>
#include <stdint.h>

typedef __attribute__((ext_vector_type(8))) short bf16x8;
typedef __attribute__((ext_vector_type(4))) short s16x4;
typedef __attribute__((ext_vector_type(4))) float f32x4;
typedef __attribute__((ext_vector_type(4))) unsigned u32x4;
typedef __attribute__((ext_vector_type(2))) unsigned u32x2;
#define LDSP(T) __attribute__((address_space(3))) T*
#define DI __device__ __forceinline__

constexpr int NB = 8, SQ = 112, DM = 640, NH = 32, SK = 512, DH = 128;
constexpr int SLOT = 16384;

typedef __attribute__((ext_vector_type(2))) float f32x2;
typedef __attribute__((ext_vector_type(2))) __bf16 bf16x2;
DI unsigned cvtpk(float lo, float hi) {
  f32x2 v = {lo, hi};
  return __builtin_bit_cast(unsigned, __builtin_convertvector(v, bf16x2));
}
DI f32x4 mfma16(bf16x8 a, bf16x8 b, f32x4 c) { return __builtin_amdgcn_mfma_f32_16x16x32_bf16(a, b, c, 0, 0, 0); }
DI s16x4 trread(unsigned addr) { return __builtin_amdgcn_ds_read_tr16_b64_v4i16((LDSP(s16x4))(size_t)addr); }
DI bf16x8 trpair(unsigned a0, unsigned a1) {
  s16x4 lo = trread(a0), hi = trread(a1);
  return __builtin_shufflevector(lo, hi, 0, 1, 2, 3, 4, 5, 6, 7);
}
DI bf16x8 ldsread16(unsigned addr) { return *(LDSP(bf16x8))(size_t)addr; }
DI void ldswrite16(unsigned addr, bf16x8 v) { *(LDSP(bf16x8))(size_t)addr = v; }

DI void dma1(unsigned m0val, unsigned voff, const void* sbase) {
  unsigned keep;
  asm volatile("s_mov_b32 %0, m0\n\ts_mov_b32 m0, %1\n\ts_nop 0\n\t"
               "global_load_lds_dwordx4 %2, %3\n\t"
               "s_mov_b32 m0, %0"
               : "=&s"(keep) : "s"(m0val), "v"(voff), "s"(sbase) : "memory");
}
#define BARRIER() asm volatile("s_waitcnt lgkmcnt(0)\n\ts_barrier" ::: "memory")
template <int N> DI void waitv() { asm volatile("s_waitcnt vmcnt(%0)" ::"i"(N) : "memory"); }
#define OPAQUE(x) asm volatile("" : "+v"(x))
#define SCHED() __builtin_amdgcn_sched_barrier(0)

typedef __attribute__((ext_vector_type(16))) float f32x16;
DI f32x16 mfma32(bf16x8 a, bf16x8 b, f32x16 c) { return __builtin_amdgcn_mfma_f32_32x32x16_bf16(a, b, c, 0, 0, 0); }
DI void dma1nt(unsigned m0val, unsigned voff, const void* sbase) {
  unsigned keep;
  asm volatile("s_mov_b32 %0, m0\n\ts_mov_b32 m0, %1\n\ts_nop 0\n\t"
               "global_load_lds_dwordx4 %2, %3 nt\n\t"
               "s_mov_b32 m0, %0"
               : "=&s"(keep) : "s"(m0val), "v"(voff), "s"(sbase) : "memory");
}
DI void dma1sc(unsigned m0val, unsigned voff, const void* sbase) {
  unsigned keep;
  asm volatile("s_mov_b32 %0, m0\n\ts_mov_b32 m0, %1\n\ts_nop 0\n\t"
               "global_load_lds_dwordx4 %2, %3 sc1\n\t"
               "s_mov_b32 m0, %0"
               : "=&s"(keep) : "s"(m0val), "v"(voff), "s"(sbase) : "memory");
}
DI f32x4 ldsreadf(unsigned addr) { return *(LDSP(f32x4))(size_t)addr; }
DI void ldswritef(unsigned addr, f32x4 v) { *(LDSP(f32x4))(size_t)addr = v; }

__host__ __device__ constexpr int vslot(int t) { return t < 2 ? 8 + t : t - 2; }

DI void attn_body(const uint16_t* __restrict__ x, const uint16_t* __restrict__ kc, const uint16_t* __restrict__ vc,
                  const uint16_t* __restrict__ wq, uint16_t* __restrict__ ows, unsigned lds0, int wid, int lane, int b, int h, unsigned& t0, unsigned& t1, unsigned& t2) {
  OPAQUE(lane);
  const int r = lane & 15, g = lane >> 4;
  const int q = lane & 31, hh = lane >> 5;
  const int q4 = r >> 2, p = lane & 3, blk = g & 1;
  const int qg = wid & 3, kh = wid >> 2;
  const int rot = (b + 3 * (h >> 3)) % 10;
  const uint16_t* wq_s = wq + h * DH + (size_t)(8 * wid) * 4096;
  const uint16_t* x_s = x + (size_t)(b * SQ + 16 * (wid < 7 ? wid : 6)) * DM;
  const uint16_t* kb = kc + (size_t)(b * NH + h) * SK * DH + 8 * wid * DH;
  const uint16_t* vb = vc + (size_t)(b * NH + h) * SK * DH + 8 * wid * DH;
  auto unitof = [&](int hs) { int u = hs + rot; return u >= 10 ? u - 10 : u; };
  const unsigned w_voff = (unsigned)((g * 4096 + 8 * (r ^ (g << 2))) * 2);
  const unsigned v_voff = (unsigned)((g * DH + 8 * (r ^ (g << 2))) * 2);
  const unsigned k_voff0 = (unsigned)((g * DH + 8 * (r ^ (8 * (wid & 1) + g))) * 2);
  const unsigned k_voff1 = (unsigned)(((4 + g) * DH + 8 * (r ^ (8 * (wid & 1) + 4 + g))) * 2);
  const unsigned x_voff0 = (unsigned)(((lane >> 3) * DM + 8 * ((lane & 7) ^ (lane >> 4))) * 2);
  const unsigned x_voff1 = (unsigned)(((8 + (lane >> 3)) * DM + 8 * ((lane & 7) ^ (4 | (lane >> 4)))) * 2);
  auto dmaU1 = [&](int hs, int j) {
    const unsigned d = lds0 + 2 * (hs % 5) * SLOT + wid * 2048 + (j >> 1) * 1024;
    const int u = unitof(hs);
    if ((j & 1) == 0) dma1nt(d, w_voff, wq_s + (size_t)(64 * u + 4 * (j >> 1)) * 4096);
    else dma1(d + SLOT, (j >> 1) ? x_voff1 : x_voff0, x_s + 64 * u);
  };
  auto dmaU = [&](int hs) { dmaU1(hs, 0); dmaU1(hs, 1); dmaU1(hs, 2); dmaU1(hs, 3); };
  auto dmaK1 = [&](int t, int n) { dma1nt(lds0 + t * SLOT + wid * 2048 + n * 1024, n ? k_voff1 : k_voff0, kb + t * 64 * DH); };
  auto dmaV1 = [&](int t, int n) { dma1nt(lds0 + vslot(t) * SLOT + wid * 2048 + n * 1024, v_voff, vb + (t * 64 + 4 * n) * DH); };
  dmaU(0);
  asm volatile("s_barrier" ::: "memory");
  dmaU(1);
  const unsigned tr0 = lds0 + (unsigned)((8 * hh + q4) * 256 + (q4 << 6) + 32 * blk + 8 * p);
  unsigned trb[4];
  const int xrow = 32 * qg + q;
  const unsigned xb = lds0 + SLOT + (unsigned)(xrow * 128 + ((hh ^ ((xrow >> 1) & 7)) << 4));

  f32x16 qa[2];
#pragma unroll
  for (int c = 0; c < 2; ++c)
#pragma unroll
    for (int i = 0; i < 16; ++i) qa[c][i] = 0.f;
  bf16x8 af[2][8], xf[2][4];
  auto dmaT = [&](int i) {
    if (i < 8) { dmaK1(i, 0); dmaK1(i, 1); } else if (i < 16) { dmaV1(i - 8, 0); dmaV1(i - 8, 1); }
  };
#pragma unroll
  for (int hs = 0; hs < 10; ++hs) {
    if (hs <= 8) waitv<4>(); else waitv<2>();
    if (hs == 0) asm volatile("s_waitcnt lgkmcnt(0)" : "+s"(t0), "+s"(t1), "+s"(t2) :: "memory");
    BARRIER();
#pragma unroll
    for (int c = 0; c < 2; ++c) { trb[c] = (tr0 ^ ((2 * kh + c) << 6)) + 2 * (hs % 5) * SLOT; OPAQUE(trb[c]); }
    const unsigned xa = xb + 2 * (hs % 5) * SLOT;
    SCHED();
#pragma unroll
    for (int d = 0; d < 8; ++d) {
      if (hs > 0) qa[d & 1] = mfma32(af[(hs - 1) & 1][d], xf[(hs - 1) & 1][d >> 1], qa[d & 1]);
      SCHED();
      if (d < 4) xf[hs & 1][d] = ldsread16(xa ^ (32 * d));
      if (d < 4) af[hs & 1][d] = trpair(trb[d & 1] + 4096 * (d >> 1), trb[d & 1] + 4096 * (d >> 1) + 1024);
      else if (d < 6) {
        af[hs & 1][2 * d - 4] = trpair(trb[0] + 4096 * (d - 2), trb[0] + 4096 * (d - 2) + 1024);
        af[hs & 1][2 * d - 3] = trpair(trb[1] + 4096 * (d - 2), trb[1] + 4096 * (d - 2) + 1024);
      }
      if ((d & 1) == 1 && hs + 2 < 10) dmaU1(hs + 2, d >> 1);
      if (d < 2 && hs >= 8) dmaK1(hs - 8, d);
      SCHED();
    }
  }
#pragma unroll
  for (int d = 0; d < 8; ++d) qa[d & 1] = mfma32(af[1][d], xf[1][d >> 1], qa[d & 1]);
  dmaT(2);
  constexpr float C = 0.08838834764831845f * 1.4426950408889634f;
  bf16x8 qf[8];
  {
    const unsigned mine = lds0 + 6 * SLOT + wid * 4096 + lane * 16, other = lds0 + 6 * SLOT + (wid ^ 4) * 4096 + lane * 16;
    bf16x8 qm[4];
#pragma unroll
    for (int c = 0; c < 2; ++c)
#pragma unroll
      for (int s = 0; s < 2; ++s) {
        unsigned a0 = cvtpk(qa[c][8 * s], qa[c][8 * s + 1]), a1 = cvtpk(qa[c][8 * s + 2], qa[c][8 * s + 3]);
        unsigned b0 = cvtpk(qa[c][8 * s + 4], qa[c][8 * s + 5]), b1 = cvtpk(qa[c][8 * s + 6], qa[c][8 * s + 7]);
        auto s0 = __builtin_amdgcn_permlane32_swap(a0, b0, false, false);
        auto s1 = __builtin_amdgcn_permlane32_swap(a1, b1, false, false);
        u32x4 v = {s0[0], s1[0], s0[1], s1[1]};
        qm[2 * c + s] = __builtin_bit_cast(bf16x8, v);
        ldswrite16(mine + (2 * c + s) * 1024, qm[2 * c + s]);
      }
    BARRIER();
    dmaT(3);
#pragma unroll
    for (int i = 0; i < 4; ++i) {
      const bf16x8 o = ldsread16(other + i * 1024);
      qf[i] = kh ? o : qm[i];
      qf[4 + i] = kh ? qm[i] : o;
    }
  }
  constexpr float THR = 8.0f / C;
  const int key = (q & 19) | ((q & 4) << 1) | ((q & 8) >> 1);
  const unsigned kr0 = lds0 + kh * 8192 + (unsigned)(key * 256 + ((hh ^ (key & 15)) << 4));
  unsigned krb[8];
  bf16x8 kf[10];
  f32x16 sc[2], scn, minit;
#pragma unroll
  for (int i = 0; i < 16; ++i) minit[i] = 0.f;
  bf16x8 pf[8][2];
  float alpha[8];
  unsigned resc = 0;
  float mref = 0.f, off = 0.f, thr = THR;
  auto max3 = [](float a, float b, float c) { float d; asm("v_max3_f32 %0, %1, %2, %3" : "=v"(d) : "v"(a), "v"(b), "v"(c)); return d; };
  auto sm_max = [&](int t) {
    f32x16& v = sc[t & 1];
    float m0 = max3(v[0], v[1], v[2]), m1 = max3(v[3], v[4], v[5]);
    m0 = max3(m0, v[6], v[7]); m1 = max3(m1, v[8], v[9]);
    m0 = max3(m0, v[10], v[11]); m1 = max3(m1, v[12], v[13]);
    m0 = max3(m0, v[14], v[15]);
    m0 = max3(m0, m1, m1);
    alpha[t] = 1.f;
    if (t <= 1 || __builtin_expect(__any(m0 > thr), 0)) {
      auto sw = __builtin_amdgcn_permlane32_swap(__float_as_uint(m0), __float_as_uint(m0), false, false);
      m0 = fmaxf(__uint_as_float(sw[0]), __uint_as_float(sw[1])) - off;
      const bool upd = t == 0 || __any(m0 > THR);
      const float dl = t == 0 ? m0 : (upd ? fmaxf(m0, 0.f) : 0.f);
      if (t > 0 && upd) { alpha[t] = __builtin_amdgcn_exp2f(-dl * C); resc |= 1u << t; }
      mref += dl;
      v -= off + dl;
      off = dl; thr = upd ? -3.0e38f : THR;
#pragma unroll
      for (int i = 0; i < 16; ++i) minit[i] = -mref;
    }
  };
  unsigned pw[8][8];
  float sh[8][8];
  auto sm_exp = [&](int t, int j) {
    float e[4];
#pragma unroll
    for (int i = 0; i < 4; ++i) e[i] = __builtin_amdgcn_exp2f(sc[t & 1][4 * j + i] * C);
    pw[t][2 * j] = cvtpk(e[0], e[1]);
    pw[t][2 * j + 1] = cvtpk(e[2], e[3]);
    asm volatile("" : "+v"(pw[t][2 * j]), "+v"(pw[t][2 * j + 1]));
  };
#pragma unroll
  for (int t = 0; t < 10; ++t) {
    if (t < 8) {
      waitv<6>();
      BARRIER();
      if ((t & 3) == 0) {
#pragma unroll
        for (int s = 0; s < 8; ++s) { krb[s] = (kr0 ^ (s << 5)) + t * SLOT; OPAQUE(krb[s]); }
      }
    }
    SCHED();
#pragma unroll
    for (int s = 0; s < 8; ++s) {
      if (t >= 1 && t <= 8) {
        if (s == 0) scn = mfma32(kf[(8 * (t - 1)) % 10], qf[0], minit);
        else scn = mfma32(kf[(8 * (t - 1) + s) % 10], qf[s], scn);
      }
      SCHED();
      if (t < 8) {
        if (s == 0) { kf[(8 * t) % 10] = ldsread16(krb[0] + (t & 3) * SLOT); kf[(8 * t + 1) % 10] = ldsread16(krb[1] + (t & 3) * SLOT); }
        if (s < 6) kf[(8 * t + s + 2) % 10] = ldsread16(krb[s + 2] + (t & 3) * SLOT);
      }
      if (t >= 8 && s == 1) asm volatile("s_nop 15" : "+v"(sc[t & 1]));
      if (t >= 2 && s == 1) sm_max(t - 2);
      if (t >= 2 && (s == 3 || s == 5)) sm_exp(t - 2, (s - 3) >> 1);
      if (t >= 2 && s == 2) {
#pragma unroll
        for (int i = 0; i < 8; ++i) { sh[t - 2][i] = sc[t & 1][8 + i] * C; OPAQUE(sh[t - 2][i]); }
      }
      SCHED();
    }
    if (t >= 1 && t <= 8) sc[(t - 1) & 1] = scn;
    if (t < 8) dmaT(4 + t);
  }
#pragma unroll
  for (int t = 0; t < 8; ++t) {
    u32x4 v = {pw[t][0], pw[t][1], pw[t][2], pw[t][3]};
    pf[t][0] = __builtin_bit_cast(bf16x8, v);
  }
  auto sm_exp2 = [&](int t, int j) {
    float e[4];
#pragma unroll
    for (int i = 0; i < 4; ++i) e[i] = __builtin_amdgcn_exp2f(sh[t][4 * j + i]);
    pw[t][4 + 2 * j] = cvtpk(e[0], e[1]);
    pw[t][5 + 2 * j] = cvtpk(e[2], e[3]);
    asm volatile("" : "+v"(pw[t][4 + 2 * j]), "+v"(pw[t][5 + 2 * j]));
    if (j == 1) {
      u32x4 v = {pw[t][4], pw[t][5], pw[t][6], pw[t][7]};
      pf[t][1] = __builtin_bit_cast(bf16x8, v);
    }
  };
  f32x16 oa[4];
#pragma unroll
  for (int c = 0; c < 4; ++c)
#pragma unroll
    for (int i = 0; i < 16; ++i) oa[c][i] = 0.f;
  f32x16 la;
#pragma unroll
  for (int i = 0; i < 16; ++i) la[i] = 0.f;
  const bf16x8 ones = {0x3F80, 0x3F80, 0x3F80, 0x3F80, 0x3F80, 0x3F80, 0x3F80, 0x3F80};
  bf16x8 vf[10];
  const int cs = 2 * (kh ^ 1);
#pragma unroll
  for (int t = 0; t < 8; ++t) {
    switch (t) {
      case 0: case 1: case 2: case 3: case 4: waitv<6>(); break;
      case 5: waitv<4>(); break;
      case 6: waitv<2>(); break;
      case 7: waitv<0>(); break;
    }
    BARRIER();
    if (t == 0 || t == 2 || t == 6) {
#pragma unroll
      for (int c = 0; c < 4; ++c) { trb[c] = (tr0 ^ ((c ^ cs) << 6)) + kh * 8192 + (t == 0 ? 8 * SLOT : t == 2 ? 0 : 4 * SLOT); OPAQUE(trb[c]); }
    }
    const unsigned so = (t < 2 ? t : t < 6 ? t - 2 : t - 6) * SLOT;
    if (t > 1) {
      if (__builtin_expect((resc >> (t - 1)) & 1, 0)) {
        float a = alpha[t - 1];
        OPAQUE(a);
#pragma unroll
        for (int c = 0; c < 4; ++c) oa[c] *= a;
        la *= a;
      }
    }
    SCHED();
#pragma unroll
    for (int i = 0; i < 8; ++i) {
      if (t > 0) oa[i >> 1] = mfma32(vf[(8 * (t - 1) + i) % 10], pf[t - 1][i & 1], oa[i >> 1]);
      SCHED();
      if (i == 0) {
        vf[(8 * t) % 10] = trpair(trb[0] + so, trb[0] + so + 1024);
        vf[(8 * t + 1) % 10] = trpair(trb[0] + so + 4096, trb[0] + so + 4096 + 1024);
      }
      if (i < 6) vf[(8 * t + i + 2) % 10] = trpair(trb[(i + 2) >> 1] + so + 4096 * (i & 1), trb[(i + 2) >> 1] + so + 4096 * (i & 1) + 1024);
      if (i == 2 || i == 6) sm_exp2(t, i >> 2);
      SCHED();
    }
    if (t > 0) { la = mfma32(ones, pf[t - 1][0], la); la = mfma32(ones, pf[t - 1][1], la); }
    SCHED();
    dmaT(12 + t);
  }
  if (__builtin_expect((resc >> 7) & 1, 0)) {
    float a = alpha[7];
    OPAQUE(a);
#pragma unroll
    for (int c = 0; c < 4; ++c) oa[c] *= a;
    la *= a;
  }
  la = mfma32(ones, pf[7][0], la); la = mfma32(ones, pf[7][1], la);
#pragma unroll
  for (int i = 0; i < 4; ++i) oa[i >> 1] = mfma32(vf[(56 + i) % 10], pf[7][i & 1], oa[i >> 1]);
  {
    const float l = la[0];
    const unsigned mine = lds0 + 6 * SLOT + wid * 4096 + lane * 16, other = lds0 + 6 * SLOT + (wid ^ 4) * 4096 + lane * 16;
    const unsigned mlm = lds0 + wid * 256 + q * 8, mlo = lds0 + (wid ^ 4) * 256 + q * 8;
    *(LDSP(f32x2))(size_t)mlm = f32x2{mref, l};
#pragma unroll
    for (int i = 0; i < 4; ++i) oa[2 + (i >> 1)] = mfma32(vf[(60 + i) % 10], pf[7][i & 1], oa[2 + (i >> 1)]);
#pragma unroll
    for (int c = 0; c < 2; ++c)
#pragma unroll
      for (int hf = 0; hf < 2; ++hf) {
        u32x4 v = {cvtpk(oa[c][8 * hf], oa[c][8 * hf + 1]), cvtpk(oa[c][8 * hf + 2], oa[c][8 * hf + 3]),
                   cvtpk(oa[c][8 * hf + 4], oa[c][8 * hf + 5]), cvtpk(oa[c][8 * hf + 6], oa[c][8 * hf + 7])};
        ldswrite16(mine + (2 * c + hf) * 1024, __builtin_bit_cast(bf16x8, v));
      }
    BARRIER();
    const f32x2 ml = *(LDSP(f32x2))(size_t)mlo;
    u32x4 pv[4];
#pragma unroll
    for (int i = 0; i < 4; ++i) pv[i] = __builtin_bit_cast(u32x4, ldsread16(other + i * 1024));
    const float m = fmaxf(mref, ml[0]);
    const float f0 = __builtin_amdgcn_exp2f((mref - m) * C), f1 = __builtin_amdgcn_exp2f((ml[0] - m) * C);
    const float linv = __builtin_amdgcn_rcpf(l * f0 + ml[1] * f1);
    const float s0 = f0 * linv, s1 = f1 * linv;
    uint16_t* op = ows + ((size_t)((b * 7 + 2 * qg + (q >> 4)) * 128 + h * 4 + 2 * kh) * 512) + (hh * 16 + (q & 15)) * 8;
    if (xrow < SQ) {
#pragma unroll
      for (int c = 0; c < 2; ++c)
#pragma unroll
        for (int hf = 0; hf < 2; ++hf) {
          u32x4 v;
#pragma unroll
          for (int i = 0; i < 4; ++i) {
            const unsigned w = pv[2 * c + hf][i];
            const float lo = fmaf(oa[2 + c][8 * hf + 2 * i], s0, __uint_as_float(w << 16) * s1);
            const float hi = fmaf(oa[2 + c][8 * hf + 2 * i + 1], s0, __uint_as_float(w & 0xffff0000u) * s1);
            v[i] = cvtpk(lo, hi);
          }
          asm volatile("global_store_dwordx4 %0, %1, off sc1\n\ts_nop 1" :: "v"(op + c * 512 + hf * 256), "v"(v) : "memory");
        }
    }
  }
}

extern "C" __device__ unsigned long long amd_dispatch_id() __asm("llvm.amdgcn.dispatch.id");
typedef unsigned long long u64;
constexpr int KSLOT = 13312;
constexpr size_t WS_FO = (size_t)8 << 20;
constexpr size_t WS_FX = WS_FO + 65536;
constexpr size_t WS_X = (size_t)9 << 20;
template <bool LOADER>
DI void oproj_body(const uint16_t* __restrict__ ows, const uint16_t* __restrict__ wo, uint16_t* __restrict__ out, char* __restrict__ ws,
                   unsigned lds0, int wid, int lane, u64 tag) {
  OPAQUE(lane);
  const int xcd = blockIdx.x & 7, idx = blockIdx.x >> 3;
  const int kq = xcd >> 1, mh = xcd & 1, mgi = idx / 5, nb = idx % 5;
  const int mt0 = 28 * mh + (mgi < 4 ? 5 * mgi : 20 + 4 * (mgi - 4)), mcnt = mgi < 4 ? 5 : 4;
  const int r = lane & 15, g = lane >> 4, q4 = r >> 2, p = lane & 3;
  const int rot = (mgi + nb) & 7;
  const unsigned o_voff = (unsigned)lane * 16;
  if constexpr (LOADER) {
    const int lj = wid - 4;
    const unsigned w_voff0 = (unsigned)((g * DM + 8 * (r ^ (g << 1))) * 2), w_voff1 = (unsigned)((g * DM + 8 * (r ^ ((4 | g) << 1))) * 2);
    const uint16_t* wo_s = wo + (size_t)(1024 * kq + 32 * lj) * DM + 128 * nb;
    const uint16_t* o_s = ows + ((size_t)mt0 * 128 + 32 * kq + lj) * 512;
    auto issue = [&](int u, bool dw, bool dz) {
      const int uu = (u + rot) & 7;
      const unsigned d = lds0 + ((u % 3) * 4 + lj) * KSLOT;
#pragma unroll
      for (int i = 0; i < 8; ++i) {
        if (dw) dma1(d + i * 1024, (i & 1) ? w_voff1 : w_voff0, wo_s + (size_t)(128 * uu + 4 * i) * DM);
        if (dz && i < 5) dma1sc(d + 8192 + i * 1024, o_voff, o_s + ((size_t)(i < mcnt ? i : mcnt - 1) * 128 + 4 * uu) * 512);
      }
    };
    issue(0, true, false); issue(1, true, false); issue(2, true, false);
    BARRIER();
    issue(0, false, true); issue(1, false, true); issue(2, false, true);
#pragma unroll
    for (int u = 0; u < 8; ++u) {
      if (u == 0) waitv<10>(); else if (u == 1) waitv<5>(); else if (u < 7) waitv<13>(); else waitv<0>();
      BARRIER();
      if (u >= 1 && u + 2 < 8) issue(u + 2, true, true);
    }
    return;
  } else {
  const int np = wid;
  const int trow = 16 * (g >> 1) + 4 * (g & 1) + q4;
  const unsigned tra = lds0 + (unsigned)(trow * 256 + (((2 * np) ^ (trow & 7)) << 5) + 8 * p);
  const unsigned ofa = lds0 + 8192 + lane * 16;
  f32x4 acc[2][5];
#pragma unroll
  for (int c = 0; c < 2; ++c)
#pragma unroll
    for (int m = 0; m < 5; ++m) acc[c][m] = f32x4{0.f, 0.f, 0.f, 0.f};
  bf16x8 af[2][2], bf[2][5];
  if (wid == 0) {
    const int bb = (lane & 8) ? (mt0 + mcnt - 1) / 7 : mt0 / 7;
    u64* f = (u64*)(ws + WS_FO) + bb * 32 + 8 * kq + (lane & 7);
    for (int spin = 0; spin < (1 << 22); ++spin) {
      if (__all(__hip_atomic_load(f, __ATOMIC_RELAXED, __HIP_MEMORY_SCOPE_AGENT) == tag)) break;
      __builtin_amdgcn_s_sleep(1);
    }
  }
  BARRIER();
#pragma unroll
  for (int u = 0; u < 8; ++u) {
    BARRIER();
    const unsigned ub = (u % 3) * 4 * KSLOT;
    unsigned t0 = tra + ub, t1 = (tra ^ 32) + ub, ob = ofa + ub;
    OPAQUE(t0); OPAQUE(t1); OPAQUE(ob);
    SCHED();
#pragma unroll
    for (int j = 0; j < 4; ++j) {
#pragma unroll
      for (int m = 0; m < 5; ++m) {
        if (u > 0 || j > 0) {
          acc[0][m] = mfma16(af[(j + 1) & 1][0], bf[(j + 1) & 1][m], acc[0][m]);
          acc[1][m] = mfma16(af[(j + 1) & 1][1], bf[(j + 1) & 1][m], acc[1][m]);
        }
        SCHED();
        if (m == 0) af[j & 1][0] = trpair(t0 + j * KSLOT, t0 + j * KSLOT + 2048);
        if (m == 1) af[j & 1][1] = trpair(t1 + j * KSLOT, t1 + j * KSLOT + 2048);
        if (m < 2) bf[j & 1][m] = ldsread16(ob + j * KSLOT + m * 1024);
        if (m == 2) { bf[j & 1][2] = ldsread16(ob + j * KSLOT + 2048); bf[j & 1][3] = ldsread16(ob + j * KSLOT + 3072); bf[j & 1][4] = ldsread16(ob + j * KSLOT + 4096); }
        SCHED();
      }
    }
  }
#pragma unroll
  for (int m = 0; m < 5; ++m) {
    acc[0][m] = mfma16(af[1][0], bf[1][m], acc[0][m]);
    acc[1][m] = mfma16(af[1][1], bf[1][m], acc[1][m]);
  }
  const int dest = (idx << 3) | (np << 1) | mh;
  if (np != kq) {
    char* xp = ws + WS_X + ((size_t)(dest * 4 + kq) * 5) * 1024;
#pragma unroll
    for (int m = 0; m < 5; ++m) {
      u32x4 v = {cvtpk(acc[0][m][0], acc[0][m][1]), cvtpk(acc[0][m][2], acc[0][m][3]), cvtpk(acc[1][m][0], acc[1][m][1]), cvtpk(acc[1][m][2], acc[1][m][3])};
      asm volatile("global_store_dwordx4 %0, %1, %2 offset:0 sc1\n\ts_nop 1" :: "v"(o_voff + (unsigned)(m * 1024)), "v"(v), "s"(xp) : "memory");
    }
    asm volatile("s_waitcnt vmcnt(0)" ::: "memory");
    if (lane == 0) __hip_atomic_store((u64*)(ws + WS_FX + (size_t)dest * 128) + kq, tag, __ATOMIC_RELAXED, __HIP_MEMORY_SCOPE_AGENT);
    return;
  }
  {
    u64* f = (u64*)(ws + WS_FX + (size_t)blockIdx.x * 128) + ((kq + 1 + (lane % 3)) & 3);
    for (int spin = 0; spin < (1 << 22); ++spin) {
      if (__all(__hip_atomic_load(f, __ATOMIC_RELAXED, __HIP_MEMORY_SCOPE_AGENT) == tag)) break;
      __builtin_amdgcn_s_sleep(1);
    }
    asm volatile("s_barrier" ::: "memory");
    const char* xp = ws + WS_X + ((size_t)(blockIdx.x * 4) * 5) * 1024;
    u32x4 v[3][5];
#pragma unroll
    for (int s = 0; s < 3; ++s)
#pragma unroll
      for (int m = 0; m < 5; ++m)
        asm volatile("global_load_dwordx4 %0, %1, %2 offset:0 sc1" : "=v"(v[s][m]) : "v"(o_voff + (unsigned)(m * 1024)), "s"(xp + (size_t)((kq + s + 1) & 3) * 5120) : "memory");
    asm volatile("s_waitcnt vmcnt(0)" : "+v"(v[0][0]), "+v"(v[0][1]), "+v"(v[0][2]), "+v"(v[0][3]), "+v"(v[0][4]),
                 "+v"(v[1][0]), "+v"(v[1][1]), "+v"(v[1][2]), "+v"(v[1][3]), "+v"(v[1][4]),
                 "+v"(v[2][0]), "+v"(v[2][1]), "+v"(v[2][2]), "+v"(v[2][3]), "+v"(v[2][4]) :: "memory");
#pragma unroll
    for (int m = 0; m < 5; ++m)
#pragma unroll
      for (int c = 0; c < 2; ++c) {
        f32x4 t = acc[c][m];
#pragma unroll
        for (int s = 0; s < 3; ++s) {
          const unsigned w0 = v[s][m][2 * c], w1 = v[s][m][2 * c + 1];
          t += f32x4{__uint_as_float(w0 << 16), __uint_as_float(w0 & 0xffff0000u), __uint_as_float(w1 << 16), __uint_as_float(w1 & 0xffff0000u)};
        }
        if (m < mcnt) {
          u32x2 o = {cvtpk(t[0], t[1]), cvtpk(t[2], t[3])};
          *(u32x2*)(out + (size_t)((mt0 + m) * 16 + r) * DM + 128 * nb + 16 * (2 * np + c) + 4 * g) = o;
        }
      }
  }
  }
}
__global__ __launch_bounds__(512, 2) void fused_kernel(const uint16_t* __restrict__ x, const uint16_t* __restrict__ kc,
                                                       const uint16_t* __restrict__ vc, const uint16_t* __restrict__ wq,
                                                       const uint16_t* __restrict__ wo, uint16_t* __restrict__ out, char* __restrict__ ws) {
  extern __shared__ __attribute__((aligned(16))) char smem[];
  const int lane = threadIdx.x & 63;
  const int wid = __builtin_amdgcn_readfirstlane(threadIdx.x >> 6);
  const int h = blockIdx.x & 31, b = blockIdx.x >> 5;
  const unsigned lds0 = (unsigned)(size_t)smem;
  const u64 tag = (amd_dispatch_id() + 1) * 0x9E3779B97F4A7C15ull + (u64)(uintptr_t)__builtin_amdgcn_dispatch_ptr() * 0xC2B2AE3D27D4EB4Full + 0x165667B19E3779F9ull;
  unsigned t0, t1, t2;
  {
    const int wi = (blockIdx.x >> 3) * 8 + wid;
    const char* pa = wi < 80 ? (const char*)wq + (size_t)wi * 65536 : wi < 160 ? (const char*)wo + (size_t)(wi - 80) * 65536
                   : wi < 177 ? (const char*)x + (size_t)(wi - 160) * 65536 : wi < 194 ? (const char*)out + (size_t)(wi - 177) * 65536
                   : ws + WS_FO + (size_t)(wi - 194) * 98304;
    const char* pb = wid < 2 ? (const char*)(kc + (size_t)blockIdx.x * SK * DH) + wid * 65536
                   : wid < 4 ? (const char*)(vc + (size_t)blockIdx.x * SK * DH) + (wid - 2) * 65536
                   : ws + (size_t)((blockIdx.x >> 3) * 4 + wid - 4) * 57344;
    asm volatile("s_load_dword %0, %2, 0x0\n\ts_load_dword %1, %3, 0x0" : "=&s"(t0), "=&s"(t1) : "s"(pa), "s"(pb) : "memory");
    u64 pc;
    asm volatile("s_getpc_b64 %0" : "=s"(pc));
    pc = (pc & ~(u64)127) + (u64)(wi < 240 ? wi : 239) * 128;
    asm volatile("s_load_dword %0, %1, 0x0" : "=&s"(t2) : "s"(pc) : "memory");
  }
  attn_body(x, kc, vc, wq, (uint16_t*)ws, lds0, wid, lane, b, h, t0, t1, t2);
  asm volatile("s_waitcnt vmcnt(0)" ::: "memory");
  BARRIER();
  if (threadIdx.x == 0) __hip_atomic_store((u64*)(ws + WS_FO) + blockIdx.x, tag, __ATOMIC_RELAXED, __HIP_MEMORY_SCOPE_AGENT);
  if (blockIdx.x >= 240) return;
  if (wid < 4) oproj_body<false>((const uint16_t*)ws, wo, out, ws, lds0, wid, lane, tag);
  else oproj_body<true>((const uint16_t*)ws, wo, out, ws, lds0, wid, lane, tag);
}

extern "C" void kernel_launch(void* const* d_in, const int* in_sizes, int n_in, void* d_out, int out_size, void* d_ws,
                              size_t ws_size, hipStream_t stream) {
  const uint16_t* x = (const uint16_t*)d_in[0];
  const uint16_t* kc = (const uint16_t*)d_in[1];
  const uint16_t* vc = (const uint16_t*)d_in[2];
  const uint16_t* wq = (const uint16_t*)d_in[3];
  const uint16_t* wo = (const uint16_t*)d_in[4];
  (void)hipFuncSetAttribute((const void*)fused_kernel, hipFuncAttributeMaxDynamicSharedMemorySize, 10 * SLOT);
  fused_kernel<<<256, 512, 10 * SLOT, stream>>>(x, kc, vc, wq, wo, (uint16_t*)d_out, (char*)d_ws);
}
```

```cpp
#include <hip/hip_runtime.h>
#include <hip/hip_bf16.h>
#include <stdint.h>

typedef __attribute__((ext_vector_type(8))) short bf16x8;
typedef __attribute__((ext_vector_type(4))) short s16x4;
typedef __attribute__((ext_vector_type(4))) float f32x4;
typedef __attribute__((ext_vector_type(4))) unsigned u32x4;
typedef __attribute__((ext_vector_type(2))) unsigned u32x2;
#define LDSP(T) __attribute__((address_space(3))) T*
#define DI __device__ __forceinline__

constexpr int NB = 8, SQ = 112, DM = 640, NH = 32, SK = 512, DH = 128;
constexpr int SLOT = 16384;

typedef __attribute__((ext_vector_type(2))) float f32x2;
typedef __attribute__((ext_vector_type(2))) __bf16 bf16x2;
DI unsigned cvtpk(float lo, float hi) {
  f32x2 v = {lo, hi};
  return __builtin_bit_cast(unsigned, __builtin_convertvector(v, bf16x2));
}
DI f32x4 mfma16(bf16x8 a, bf16x8 b, f32x4 c) { return __builtin_amdgcn_mfma_f32_16x16x32_bf16(a, b, c, 0, 0, 0); }
DI s16x4 trread(unsigned addr) { return __builtin_amdgcn_ds_read_tr16_b64_v4i16((LDSP(s16x4))(size_t)addr); }
DI bf16x8 trpair(unsigned a0, unsigned a1) {
  s16x4 lo = trread(a0), hi = trread(a1);
  return __builtin_shufflevector(lo, hi, 0, 1, 2, 3, 4, 5, 6, 7);
}
DI bf16x8 ldsread16(unsigned addr) { return *(LDSP(bf16x8))(size_t)addr; }
DI void ldswrite16(unsigned addr, bf16x8 v) { *(LDSP(bf16x8))(size_t)addr = v; }

DI void dma1(unsigned m0val, unsigned voff, const void* sbase) {
  unsigned keep;
  asm volatile("s_mov_b32 %0, m0\n\ts_mov_b32 m0, %1\n\ts_nop 0\n\t"
               "global_load_lds_dwordx4 %2, %3\n\t"
               "s_mov_b32 m0, %0"
               : "=&s"(keep) : "s"(m0val), "v"(voff), "s"(sbase) : "memory");
}
#define BARRIER() asm volatile("s_waitcnt lgkmcnt(0)\n\ts_barrier" ::: "memory")
template <int N> DI void waitv() { asm volatile("s_waitcnt vmcnt(%0)" ::"i"(N) : "memory"); }
#define OPAQUE(x) asm volatile("" : "+v"(x))
#define SCHED() __builtin_amdgcn_sched_barrier(0)

typedef __attribute__((ext_vector_type(16))) float f32x16;
DI f32x16 mfma32(bf16x8 a, bf16x8 b, f32x16 c) { return __builtin_amdgcn_mfma_f32_32x32x16_bf16(a, b, c, 0, 0, 0); }
DI void dma1nt(unsigned m0val, unsigned voff, const void* sbase) {
  unsigned keep;
  asm volatile("s_mov_b32 %0, m0\n\ts_mov_b32 m0, %1\n\ts_nop 0\n\t"
               "global_load_lds_dwordx4 %2, %3 nt\n\t"
               "s_mov_b32 m0, %0"
               : "=&s"(keep) : "s"(m0val), "v"(voff), "s"(sbase) : "memory");
}
DI void dma1sc(unsigned m0val, unsigned voff, const void* sbase) {
  unsigned keep;
  asm volatile("s_mov_b32 %0, m0\n\ts_mov_b32 m0, %1\n\ts_nop 0\n\t"
               "global_load_lds_dwordx4 %2, %3 sc1\n\t"
               "s_mov_b32 m0, %0"
               : "=&s"(keep) : "s"(m0val), "v"(voff), "s"(sbase) : "memory");
}
DI f32x4 ldsreadf(unsigned addr) { return *(LDSP(f32x4))(size_t)addr; }
DI void ldswritef(unsigned addr, f32x4 v) { *(LDSP(f32x4))(size_t)addr = v; }

__host__ __device__ constexpr int vslot(int t) { return t < 2 ? 8 + t : t - 2; }

DI void attn_body(const uint16_t* __restrict__ x, const uint16_t* __restrict__ kc, const uint16_t* __restrict__ vc,
                  const uint16_t* __restrict__ wq, uint16_t* __restrict__ ows, unsigned lds0, int wid, int lane, int b, int h) {
  OPAQUE(lane);
  const int r = lane & 15, g = lane >> 4;
  const int q = lane & 31, hh = lane >> 5;
  const int q4 = r >> 2, p = lane & 3, blk = g & 1;
  const int qg = wid & 3, kh = wid >> 2;
  const int rot = (b + 3 * (h >> 3)) % 10;
  const uint16_t* wq_s = wq + h * DH + (size_t)(8 * wid) * 4096;
  const uint16_t* x_s = x + (size_t)(b * SQ + 16 * (wid < 7 ? wid : 6)) * DM;
  const uint16_t* kb = kc + (size_t)(b * NH + h) * SK * DH + 8 * wid * DH;
  const uint16_t* vb = vc + (size_t)(b * NH + h) * SK * DH + 8 * wid * DH;
  auto unitof = [&](int hs) { int u = hs + rot; return u >= 10 ? u - 10 : u; };
  const unsigned w_voff = (unsigned)((g * 4096 + 8 * (r ^ (g << 2))) * 2);
  const unsigned v_voff = (unsigned)((g * DH + 8 * (r ^ (g << 2))) * 2);
  const unsigned k_voff0 = (unsigned)((g * DH + 8 * (r ^ (8 * (wid & 1) + g))) * 2);
  const unsigned k_voff1 = (unsigned)(((4 + g) * DH + 8 * (r ^ (8 * (wid & 1) + 4 + g))) * 2);
  const unsigned x_voff0 = (unsigned)(((lane >> 3) * DM + 8 * ((lane & 7) ^ (lane >> 4))) * 2);
  const unsigned x_voff1 = (unsigned)(((8 + (lane >> 3)) * DM + 8 * ((lane & 7) ^ (4 | (lane >> 4)))) * 2);
  auto dmaU1 = [&](int hs, int j) {
    const unsigned d = lds0 + 2 * (hs % 5) * SLOT + wid * 2048 + (j >> 1) * 1024;
    const int u = unitof(hs);
    if ((j & 1) == 0) dma1(d, w_voff, wq_s + (size_t)(64 * u + 4 * (j >> 1)) * 4096);
    else dma1(d + SLOT, (j >> 1) ? x_voff1 : x_voff0, x_s + 64 * u);
  };
  auto dmaU = [&](int hs) { dmaU1(hs, 0); dmaU1(hs, 1); dmaU1(hs, 2); dmaU1(hs, 3); };
  auto dmaK1 = [&](int t, int n) { dma1nt(lds0 + t * SLOT + wid * 2048 + n * 1024, n ? k_voff1 : k_voff0, kb + t * 64 * DH); };
  auto dmaV1 = [&](int t, int n) { dma1nt(lds0 + vslot(t) * SLOT + wid * 2048 + n * 1024, v_voff, vb + (t * 64 + 4 * n) * DH); };
  dmaU(0);
  asm volatile("s_barrier" ::: "memory");
  dmaU(1);
  const unsigned tr0 = lds0 + (unsigned)((8 * hh + q4) * 256 + (q4 << 6) + 32 * blk + 8 * p);
  unsigned trb[4];
  const int xrow = 32 * qg + q;
  const unsigned xb = lds0 + SLOT + (unsigned)(xrow * 128 + ((hh ^ ((xrow >> 1) & 7)) << 4));

  f32x16 qa[2];
#pragma unroll
  for (int c = 0; c < 2; ++c)
#pragma unroll
    for (int i = 0; i < 16; ++i) qa[c][i] = 0.f;
  bf16x8 af[2][8], xf[2][4];
  auto dmaT = [&](int i) {
    if (i < 8) { dmaK1(i, 0); dmaK1(i, 1); } else if (i < 16) { dmaV1(i - 8, 0); dmaV1(i - 8, 1); }
  };
#pragma unroll
  for (int hs = 0; hs < 10; ++hs) {
    if (hs <= 8) waitv<4>(); else waitv<2>();
    BARRIER();
#pragma unroll
    for (int c = 0; c < 2; ++c) { trb[c] = (tr0 ^ ((2 * kh + c) << 6)) + 2 * (hs % 5) * SLOT; OPAQUE(trb[c]); }
    const unsigned xa = xb + 2 * (hs % 5) * SLOT;
    SCHED();
#pragma unroll
    for (int d = 0; d < 8; ++d) {
      if (hs > 0) qa[d & 1] = mfma32(af[(hs - 1) & 1][d], xf[(hs - 1) & 1][d >> 1], qa[d & 1]);
      SCHED();
      if (d < 4) xf[hs & 1][d] = ldsread16(xa ^ (32 * d));
      if (d < 4) af[hs & 1][d] = trpair(trb[d & 1] + 4096 * (d >> 1), trb[d & 1] + 4096 * (d >> 1) + 1024);
      else if (d < 6) {
        af[hs & 1][2 * d - 4] = trpair(trb[0] + 4096 * (d - 2), trb[0] + 4096 * (d - 2) + 1024);
        af[hs & 1][2 * d - 3] = trpair(trb[1] + 4096 * (d - 2), trb[1] + 4096 * (d - 2) + 1024);
      }
      if ((d & 1) == 1 && hs + 2 < 10) dmaU1(hs + 2, d >> 1);
      if (d < 2 && hs >= 8) dmaK1(hs - 8, d);
      SCHED();
    }
  }
#pragma unroll
  for (int d = 0; d < 8; ++d) qa[d & 1] = mfma32(af[1][d], xf[1][d >> 1], qa[d & 1]);
  dmaT(2);
  constexpr float C = 0.08838834764831845f * 1.4426950408889634f;
  bf16x8 qf[8];
  {
    const unsigned mine = lds0 + 6 * SLOT + wid * 4096 + lane * 16, other = lds0 + 6 * SLOT + (wid ^ 4) * 4096 + lane * 16;
    bf16x8 qm[4];
#pragma unroll
    for (int c = 0; c < 2; ++c)
#pragma unroll
      for (int s = 0; s < 2; ++s) {
        unsigned a0 = cvtpk(qa[c][8 * s], qa[c][8 * s + 1]), a1 = cvtpk(qa[c][8 * s + 2], qa[c][8 * s + 3]);
        unsigned b0 = cvtpk(qa[c][8 * s + 4], qa[c][8 * s + 5]), b1 = cvtpk(qa[c][8 * s + 6], qa[c][8 * s + 7]);
        auto s0 = __builtin_amdgcn_permlane32_swap(a0, b0, false, false);
        auto s1 = __builtin_amdgcn_permlane32_swap(a1, b1, false, false);
        u32x4 v = {s0[0], s1[0], s0[1], s1[1]};
        qm[2 * c + s] = __builtin_bit_cast(bf16x8, v);
        ldswrite16(mine + (2 * c + s) * 1024, qm[2 * c + s]);
      }
    BARRIER();
    dmaT(3);
#pragma unroll
    for (int i = 0; i < 4; ++i) {
      const bf16x8 o = ldsread16(other + i * 1024);
      qf[i] = kh ? o : qm[i];
      qf[4 + i] = kh ? qm[i] : o;
    }
  }
  constexpr float THR = 8.0f / C;
  const int key = (q & 19) | ((q & 4) << 1) | ((q & 8) >> 1);
  const unsigned kr0 = lds0 + kh * 8192 + (unsigned)(key * 256 + ((hh ^ (key & 15)) << 4));
  unsigned krb[8];
  bf16x8 kf[10];
  f32x16 sc[2], scn, minit;
#pragma unroll
  for (int i = 0; i < 16; ++i) minit[i] = 0.f;
  bf16x8 pf[8][2];
  float alpha[8];
  unsigned resc = 0;
  float mref = 0.f, off = 0.f, thr = THR;
  auto max3 = [](float a, float b, float c) { float d; asm volatile("v_max3_f32 %0, %1, %2, %3" : "=v"(d) : "v"(a), "v"(b), "v"(c)); return d; };
  auto sm_max = [&](int t) {
    f32x16& v = sc[t & 1];
    float m0 = max3(v[0], v[1], v[2]), m1 = max3(v[3], v[4], v[5]);
    m0 = max3(m0, v[6], v[7]); m1 = max3(m1, v[8], v[9]);
    m0 = max3(m0, v[10], v[11]); m1 = max3(m1, v[12], v[13]);
    m0 = max3(m0, v[14], v[15]);
    m0 = max3(m0, m1, m1);
    alpha[t] = 1.f;
    if (t <= 1 || __builtin_expect(__any(m0 > thr), 0)) {
      auto sw = __builtin_amdgcn_permlane32_swap(__float_as_uint(m0), __float_as_uint(m0), false, false);
      m0 = fmaxf(__uint_as_float(sw[0]), __uint_as_float(sw[1])) - off;
      const bool upd = t == 0 || __any(m0 > THR);
      const float dl = t == 0 ? m0 : (upd ? fmaxf(m0, 0.f) : 0.f);
      if (t > 0 && upd) { alpha[t] = __builtin_amdgcn_exp2f(-dl * C); resc |= 1u << t; }
      mref += dl;
      v -= off + dl;
      off = dl; thr = upd ? -3.0e38f : THR;
#pragma unroll
      for (int i = 0; i < 16; ++i) minit[i] = -mref;
    }
  };
  unsigned pw[8][8];
  float sh[8][8];
  auto sm_exp = [&](int t, int j) {
    float e[4];
#pragma unroll
    for (int i = 0; i < 4; ++i) e[i] = __builtin_amdgcn_exp2f(sc[t & 1][4 * j + i] * C);
    pw[t][2 * j] = cvtpk(e[0], e[1]);
    pw[t][2 * j + 1] = cvtpk(e[2], e[3]);
    asm volatile("" : "+v"(pw[t][2 * j]), "+v"(pw[t][2 * j + 1]));
  };
#pragma unroll
  for (int t = 0; t < 10; ++t) {
    if (t < 8) {
      waitv<6>();
      BARRIER();
      if ((t & 3) == 0) {
#pragma unroll
        for (int s = 0; s < 8; ++s) { krb[s] = (kr0 ^ (s << 5)) + t * SLOT; OPAQUE(krb[s]); }
      }
    }
    SCHED();
#pragma unroll
    for (int s = 0; s < 8; ++s) {
      if (t >= 1 && t <= 8) {
        if (s == 0) scn = mfma32(kf[(8 * (t - 1)) % 10], qf[0], minit);
        else scn = mfma32(kf[(8 * (t - 1) + s) % 10], qf[s], scn);
      }
      SCHED();
      if (t < 8) {
        if (s == 0) { kf[(8 * t) % 10] = ldsread16(krb[0] + (t & 3) * SLOT); kf[(8 * t + 1) % 10] = ldsread16(krb[1] + (t & 3) * SLOT); }
        if (s < 6) kf[(8 * t + s + 2) % 10] = ldsread16(krb[s + 2] + (t & 3) * SLOT);
      }
      if (t >= 8 && s == 1) asm volatile("s_nop 15" : "+v"(sc[t & 1]));
      if (t >= 2 && s == 1) sm_max(t - 2);
      if (t >= 2 && (s == 3 || s == 5)) sm_exp(t - 2, (s - 3) >> 1);
      if (t >= 2 && s == 2) {
#pragma unroll
        for (int i = 0; i < 8; ++i) { sh[t - 2][i] = sc[t & 1][8 + i] * C; OPAQUE(sh[t - 2][i]); }
      }
      SCHED();
    }
    if (t >= 1 && t <= 8) sc[(t - 1) & 1] = scn;
    if (t < 8) dmaT(4 + t);
  }
#pragma unroll
  for (int t = 0; t < 8; ++t) {
    u32x4 v = {pw[t][0], pw[t][1], pw[t][2], pw[t][3]};
    pf[t][0] = __builtin_bit_cast(bf16x8, v);
  }
  auto sm_exp2 = [&](int t, int j) {
    float e[4];
#pragma unroll
    for (int i = 0; i < 4; ++i) e[i] = __builtin_amdgcn_exp2f(sh[t][4 * j + i]);
    pw[t][4 + 2 * j] = cvtpk(e[0], e[1]);
    pw[t][5 + 2 * j] = cvtpk(e[2], e[3]);
    asm volatile("" : "+v"(pw[t][4 + 2 * j]), "+v"(pw[t][5 + 2 * j]));
    if (j == 1) {
      u32x4 v = {pw[t][4], pw[t][5], pw[t][6], pw[t][7]};
      pf[t][1] = __builtin_bit_cast(bf16x8, v);
    }
  };
  f32x16 oa[4];
#pragma unroll
  for (int c = 0; c < 4; ++c)
#pragma unroll
    for (int i = 0; i < 16; ++i) oa[c][i] = 0.f;
  f32x16 la;
#pragma unroll
  for (int i = 0; i < 16; ++i) la[i] = 0.f;
  const bf16x8 ones = {0x3F80, 0x3F80, 0x3F80, 0x3F80, 0x3F80, 0x3F80, 0x3F80, 0x3F80};
  bf16x8 vf[10];
  const int cs = 2 * (kh ^ 1);
#pragma unroll
  for (int t = 0; t < 8; ++t) {
    switch (t) {
      case 0: case 1: case 2: case 3: case 4: waitv<6>(); break;
      case 5: waitv<4>(); break;
      case 6: waitv<2>(); break;
      case 7: waitv<0>(); break;
    }
    BARRIER();
    if (t == 0 || t == 2 || t == 6) {
#pragma unroll
      for (int c = 0; c < 4; ++c) { trb[c] = (tr0 ^ ((c ^ cs) << 6)) + kh * 8192 + (t == 0 ? 8 * SLOT : t == 2 ? 0 : 4 * SLOT); OPAQUE(trb[c]); }
    }
    const unsigned so = (t < 2 ? t : t < 6 ? t - 2 : t - 6) * SLOT;
    if (t > 1) {
      if (__builtin_expect((resc >> (t - 1)) & 1, 0)) {
        float a = alpha[t - 1];
        OPAQUE(a);
#pragma unroll
        for (int c = 0; c < 4; ++c) oa[c] *= a;
        la *= a;
      }
    }
    SCHED();
#pragma unroll
    for (int i = 0; i < 8; ++i) {
      if (t > 0) oa[i >> 1] = mfma32(vf[(8 * (t - 1) + i) % 10], pf[t - 1][i & 1], oa[i >> 1]);
      SCHED();
      if (i == 0) {
        vf[(8 * t) % 10] = trpair(trb[0] + so, trb[0] + so + 1024);
        vf[(8 * t + 1) % 10] = trpair(trb[0] + so + 4096, trb[0] + so + 4096 + 1024);
      }
      if (i < 6) vf[(8 * t + i + 2) % 10] = trpair(trb[(i + 2) >> 1] + so + 4096 * (i & 1), trb[(i + 2) >> 1] + so + 4096 * (i & 1) + 1024);
      if (i == 2 || i == 6) sm_exp2(t, i >> 2);
      SCHED();
    }
    if (t > 0) { la = mfma32(ones, pf[t - 1][0], la); la = mfma32(ones, pf[t - 1][1], la); }
    SCHED();
    dmaT(12 + t);
  }
  if (__builtin_expect((resc >> 7) & 1, 0)) {
    float a = alpha[7];
    OPAQUE(a);
#pragma unroll
    for (int c = 0; c < 4; ++c) oa[c] *= a;
    la *= a;
  }
  la = mfma32(ones, pf[7][0], la); la = mfma32(ones, pf[7][1], la);
#pragma unroll
  for (int i = 0; i < 4; ++i) oa[i >> 1] = mfma32(vf[(56 + i) % 10], pf[7][i & 1], oa[i >> 1]);
  {
    const float l = la[0];
    const unsigned mine = lds0 + 6 * SLOT + wid * 4096 + lane * 16, other = lds0 + 6 * SLOT + (wid ^ 4) * 4096 + lane * 16;
    const unsigned mlm = lds0 + wid * 256 + q * 8, mlo = lds0 + (wid ^ 4) * 256 + q * 8;
    *(LDSP(f32x2))(size_t)mlm = f32x2{mref, l};
#pragma unroll
    for (int i = 0; i < 4; ++i) oa[2 + (i >> 1)] = mfma32(vf[(60 + i) % 10], pf[7][i & 1], oa[2 + (i >> 1)]);
#pragma unroll
    for (int c = 0; c < 2; ++c)
#pragma unroll
      for (int hf = 0; hf < 2; ++hf) {
        u32x4 v = {cvtpk(oa[c][8 * hf], oa[c][8 * hf + 1]), cvtpk(oa[c][8 * hf + 2], oa[c][8 * hf + 3]),
                   cvtpk(oa[c][8 * hf + 4], oa[c][8 * hf + 5]), cvtpk(oa[c][8 * hf + 6], oa[c][8 * hf + 7])};
        ldswrite16(mine + (2 * c + hf) * 1024, __builtin_bit_cast(bf16x8, v));
      }
    BARRIER();
    const f32x2 ml = *(LDSP(f32x2))(size_t)mlo;
    u32x4 pv[4];
#pragma unroll
    for (int i = 0; i < 4; ++i) pv[i] = __builtin_bit_cast(u32x4, ldsread16(other + i * 1024));
    const float m = fmaxf(mref, ml[0]);
    const float f0 = __builtin_amdgcn_exp2f((mref - m) * C), f1 = __builtin_amdgcn_exp2f((ml[0] - m) * C);
    const float linv = __builtin_amdgcn_rcpf(l * f0 + ml[1] * f1);
    const float s0 = f0 * linv, s1 = f1 * linv;
    uint16_t* op = ows + ((size_t)((b * 7 + 2 * qg + (q >> 4)) * 128 + h * 4 + 2 * kh) * 512) + (hh * 16 + (q & 15)) * 8;
    if (xrow < SQ) {
#pragma unroll
      for (int c = 0; c < 2; ++c)
#pragma unroll
        for (int hf = 0; hf < 2; ++hf) {
          u32x4 v;
#pragma unroll
          for (int i = 0; i < 4; ++i) {
            const unsigned w = pv[2 * c + hf][i];
            const float lo = fmaf(oa[2 + c][8 * hf + 2 * i], s0, __uint_as_float(w << 16) * s1);
            const float hi = fmaf(oa[2 + c][8 * hf + 2 * i + 1], s0, __uint_as_float(w & 0xffff0000u) * s1);
            v[i] = cvtpk(lo, hi);
          }
          asm volatile("global_store_dwordx4 %0, %1, off sc1\n\ts_nop 1" :: "v"(op + c * 512 + hf * 256), "v"(v) : "memory");
        }
    }
  }
}

extern "C" __device__ unsigned long long amd_dispatch_id() __asm("llvm.amdgcn.dispatch.id");
typedef unsigned long long u64;
constexpr int KSLOT = 13312;
constexpr size_t WS_FO = (size_t)8 << 20;
constexpr size_t WS_FX = WS_FO + 65536;
constexpr size_t WS_X = (size_t)9 << 20;
template <bool LOADER>
DI void oproj_body(const uint16_t* __restrict__ ows, const uint16_t* __restrict__ wo, uint16_t* __restrict__ out, char* __restrict__ ws,
                   unsigned lds0, int wid, int lane, u64 tag) {
  OPAQUE(lane);
  const int xcd = blockIdx.x & 7, idx = blockIdx.x >> 3;
  const int kq = xcd >> 1, mh = xcd & 1, mgi = idx / 5, nb = idx % 5;
  const int mt0 = 28 * mh + (mgi < 4 ? 5 * mgi : 20 + 4 * (mgi - 4)), mcnt = mgi < 4 ? 5 : 4;
  const int r = lane & 15, g = lane >> 4, q4 = r >> 2, p = lane & 3;
  const int rot = (mgi + nb) & 7;
  const unsigned o_voff = (unsigned)lane * 16;
  if constexpr (LOADER) {
    const int lj = wid - 4;
    const unsigned w_voff0 = (unsigned)((g * DM + 8 * (r ^ (g << 1))) * 2), w_voff1 = (unsigned)((g * DM + 8 * (r ^ ((4 | g) << 1))) * 2);
    const uint16_t* wo_s = wo + (size_t)(1024 * kq + 32 * lj) * DM + 128 * nb;
    const uint16_t* o_s = ows + ((size_t)mt0 * 128 + 32 * kq + lj) * 512;
    auto issue = [&](int u, bool dw, bool dz) {
      const int uu = (u + rot) & 7;
      const unsigned d = lds0 + ((u % 3) * 4 + lj) * KSLOT;
#pragma unroll
      for (int i = 0; i < 8; ++i) {
        if (dw) dma1(d + i * 1024, (i & 1) ? w_voff1 : w_voff0, wo_s + (size_t)(128 * uu + 4 * i) * DM);
        if (dz && i < 5) dma1sc(d + 8192 + i * 1024, o_voff, o_s + ((size_t)(i < mcnt ? i : mcnt - 1) * 128 + 4 * uu) * 512);
      }
    };
    issue(0, true, false); issue(1, true, false); issue(2, true, false);
    BARRIER();
    issue(0, false, true); issue(1, false, true); issue(2, false, true);
#pragma unroll
    for (int u = 0; u < 8; ++u) {
      if (u == 0) waitv<10>(); else if (u == 1) waitv<5>(); else if (u < 7) waitv<13>(); else waitv<0>();
      BARRIER();
      if (u >= 1 && u + 2 < 8) issue(u + 2, true, true);
    }
    return;
  } else {
  const int np = wid;
  const int trow = 16 * (g >> 1) + 4 * (g & 1) + q4;
  const unsigned tra = lds0 + (unsigned)(trow * 256 + (((2 * np) ^ (trow & 7)) << 5) + 8 * p);
  const unsigned ofa = lds0 + 8192 + lane * 16;
  f32x4 acc[2][5];
#pragma unroll
  for (int c = 0; c < 2; ++c)
#pragma unroll
    for (int m = 0; m < 5; ++m) acc[c][m] = f32x4{0.f, 0.f, 0.f, 0.f};
  bf16x8 af[2][2], bf[2][5];
  if (wid == 0) {
    const int bb = (lane & 8) ? (mt0 + mcnt - 1) / 7 : mt0 / 7;
    u64* f = (u64*)(ws + WS_FO) + bb * 32 + 8 * kq + (lane & 7);
    for (int spin = 0; spin < (1 << 18); ++spin) {
      if (__all(__hip_atomic_load(f, __ATOMIC_RELAXED, __HIP_MEMORY_SCOPE_AGENT) == tag)) break;
      __builtin_amdgcn_s_sleep(1);
    }
  }
  BARRIER();
#pragma unroll
  for (int u = 0; u < 8; ++u) {
    BARRIER();
    const unsigned ub = (u % 3) * 4 * KSLOT;
    unsigned t0 = tra + ub, t1 = (tra ^ 32) + ub, ob = ofa + ub;
    OPAQUE(t0); OPAQUE(t1); OPAQUE(ob);
    SCHED();
#pragma unroll
    for (int j = 0; j < 4; ++j) {
#pragma unroll
      for (int m = 0; m < 5; ++m) {
        if (u > 0 || j > 0) {
          acc[0][m] = mfma16(af[(j + 1) & 1][0], bf[(j + 1) & 1][m], acc[0][m]);
          acc[1][m] = mfma16(af[(j + 1) & 1][1], bf[(j + 1) & 1][m], acc[1][m]);
        }
        SCHED();
        if (m == 0) af[j & 1][0] = trpair(t0 + j * KSLOT, t0 + j * KSLOT + 2048);
        if (m == 1) af[j & 1][1] = trpair(t1 + j * KSLOT, t1 + j * KSLOT + 2048);
        if (m < 2) bf[j & 1][m] = ldsread16(ob + j * KSLOT + m * 1024);
        if (m == 2) { bf[j & 1][2] = ldsread16(ob + j * KSLOT + 2048); bf[j & 1][3] = ldsread16(ob + j * KSLOT + 3072); bf[j & 1][4] = ldsread16(ob + j * KSLOT + 4096); }
        SCHED();
      }
    }
  }
#pragma unroll
  for (int m = 0; m < 5; ++m) {
    acc[0][m] = mfma16(af[1][0], bf[1][m], acc[0][m]);
    acc[1][m] = mfma16(af[1][1], bf[1][m], acc[1][m]);
  }
  const int dest = (idx << 3) | (np << 1) | mh;
  if (np != kq) {
    char* xp = ws + WS_X + ((size_t)(dest * 4 + kq) * 5) * 1024;
#pragma unroll
    for (int m = 0; m < 5; ++m) {
      u32x4 v = {cvtpk(acc[0][m][0], acc[0][m][1]), cvtpk(acc[0][m][2], acc[0][m][3]), cvtpk(acc[1][m][0], acc[1][m][1]), cvtpk(acc[1][m][2], acc[1][m][3])};
      asm volatile("global_store_dwordx4 %0, %1, %2 offset:0 sc1\n\ts_nop 1" :: "v"(o_voff + (unsigned)(m * 1024)), "v"(v), "s"(xp) : "memory");
    }
    asm volatile("s_waitcnt vmcnt(0)" ::: "memory");
    if (lane == 0) __hip_atomic_store((u64*)(ws + WS_FX + (size_t)dest * 128) + kq, tag, __ATOMIC_RELAXED, __HIP_MEMORY_SCOPE_AGENT);
    return;
  }
  {
    u64* f = (u64*)(ws + WS_FX + (size_t)blockIdx.x * 128) + ((kq + 1 + (lane % 3)) & 3);
    for (int spin = 0; spin < (1 << 18); ++spin) {
      if (__all(__hip_atomic_load(f, __ATOMIC_RELAXED, __HIP_MEMORY_SCOPE_AGENT) == tag)) break;
      __builtin_amdgcn_s_sleep(1);
    }
    asm volatile("s_barrier" ::: "memory");
    const char* xp = ws + WS_X + ((size_t)(blockIdx.x * 4) * 5) * 1024;
    u32x4 v[3][5];
#pragma unroll
    for (int s = 0; s < 3; ++s)
#pragma unroll
      for (int m = 0; m < 5; ++m)
        asm volatile("global_load_dwordx4 %0, %1, %2 offset:0 sc1" : "=v"(v[s][m]) : "v"(o_voff + (unsigned)(m * 1024)), "s"(xp + (size_t)((kq + s + 1) & 3) * 5120) : "memory");
    asm volatile("s_waitcnt vmcnt(0)" : "+v"(v[0][0]), "+v"(v[0][1]), "+v"(v[0][2]), "+v"(v[0][3]), "+v"(v[0][4]),
                 "+v"(v[1][0]), "+v"(v[1][1]), "+v"(v[1][2]), "+v"(v[1][3]), "+v"(v[1][4]),
                 "+v"(v[2][0]), "+v"(v[2][1]), "+v"(v[2][2]), "+v"(v[2][3]), "+v"(v[2][4]) :: "memory");
#pragma unroll
    for (int m = 0; m < 5; ++m)
#pragma unroll
      for (int c = 0; c < 2; ++c) {
        f32x4 t = acc[c][m];
#pragma unroll
        for (int s = 0; s < 3; ++s) {
          const unsigned w0 = v[s][m][2 * c], w1 = v[s][m][2 * c + 1];
          t += f32x4{__uint_as_float(w0 << 16), __uint_as_float(w0 & 0xffff0000u), __uint_as_float(w1 << 16), __uint_as_float(w1 & 0xffff0000u)};
        }
        if (m < mcnt) {
          u32x2 o = {cvtpk(t[0], t[1]), cvtpk(t[2], t[3])};
          *(u32x2*)(out + (size_t)((mt0 + m) * 16 + r) * DM + 128 * nb + 16 * (2 * np + c) + 4 * g) = o;
        }
      }
  }
  }
}
__global__ __launch_bounds__(512, 2) void fused_kernel(const uint16_t* __restrict__ x, const uint16_t* __restrict__ kc,
                                                       const uint16_t* __restrict__ vc, const uint16_t* __restrict__ wq,
                                                       const uint16_t* __restrict__ wo, uint16_t* __restrict__ out, char* __restrict__ ws) {
  extern __shared__ __attribute__((aligned(16))) char smem[];
  const int lane = threadIdx.x & 63;
  const int wid = __builtin_amdgcn_readfirstlane(threadIdx.x >> 6);
  const int h = blockIdx.x & 31, b = blockIdx.x >> 5;
  const unsigned lds0 = (unsigned)(size_t)smem;
  const u64 tag = (amd_dispatch_id() + 1) * 0x9E3779B97F4A7C15ull + (u64)(uintptr_t)__builtin_amdgcn_dispatch_ptr() * 0xC2B2AE3D27D4EB4Full + 0x165667B19E3779F9ull;
  attn_body(x, kc, vc, wq, (uint16_t*)ws, lds0, wid, lane, b, h);
  asm volatile("s_waitcnt vmcnt(0)" ::: "memory");
  BARRIER();
  if (threadIdx.x == 0) __hip_atomic_store((u64*)(ws + WS_FO) + blockIdx.x, tag, __ATOMIC_RELAXED, __HIP_MEMORY_SCOPE_AGENT);
  if (blockIdx.x >= 240) return;
  if (wid < 4) oproj_body<false>((const uint16_t*)ws, wo, out, ws, lds0, wid, lane, tag);
  else oproj_body<true>((const uint16_t*)ws, wo, out, ws, lds0, wid, lane, tag);
}

extern "C" void kernel_launch(void* const* d_in, const int* in_sizes, int n_in, void* d_out, int out_size, void* d_ws,
                              size_t ws_size, hipStream_t stream) {
  const uint16_t* x = (const uint16_t*)d_in[0];
  const uint16_t* kc = (const uint16_t*)d_in[1];
  const uint16_t* vc = (const uint16_t*)d_in[2];
  const uint16_t* wq = (const uint16_t*)d_in[3];
  const uint16_t* wo = (const uint16_t*)d_in[4];
  (void)hipFuncSetAttribute((const void*)fused_kernel, hipFuncAttributeMaxDynamicSharedMemorySize, 10 * SLOT);
  fused_kernel<<<256, 512, 10 * SLOT, stream>>>(x, kc, vc, wq, wo, (uint16_t*)d_out, (char*)d_ws);
}
```
